```python
import torch

def _softmax(x, axis=-1):
    return torch.softmax(x, dim=axis)
def _gelu(x):
    return torch.nn.functional.gelu(x, approximate="tanh")
def _silu(x):
    return torch.nn.functional.silu(x)
def _sigmoid(x):
    return torch.sigmoid(x)
def _layernorm(x, axis=-1, eps=1e-5):
    m = x.mean(dim=axis, keepdim=True)
    v = x.var(dim=axis, keepdim=True, unbiased=False)
    return (x - m) / torch.sqrt(v + eps)
def _scatter_add(tgt, idx, src, axis=0):
    return tgt.clone().index_add_(axis, idx, src)
def _segment_sum(src, seg, n):
    out = torch.zeros((n,) + src.shape[1:], dtype=src.dtype)
    return out.index_add_(0, seg, src)
def _pad(x, pw):
    flat = tuple(v for pair in reversed(pw) for v in pair)
    return torch.nn.functional.pad(x, flat)

kernel_name = 'parallel_sum__ssm_conv_h3__glu_gated_swiglu__prompt'

def setup_inputs():

    _w = (lambda a: (lambda t: t.to(torch.bfloat16) if t.is_floating_point() else t)(torch.as_tensor(a)))
    def _A_setup():
        B, S, D = 1, 2048, 576
        ks = 4
        return {
            "q": _w((lambda *s: torch.randn(*s, dtype=torch.bfloat16) * 0.05)(B, S, D)),
            "k": _w((lambda *s: torch.randn(*s, dtype=torch.bfloat16) * 0.2)(B, S, D)),
            "v": _w((lambda *s: torch.randn(*s, dtype=torch.bfloat16) * 0.2)(B, S, D)),
            "shift_k": _w((lambda *s: torch.randn(*s, dtype=torch.bfloat16) * 0.2)(D, ks) * 0.5),
            "ssm_k": _w((lambda *s: torch.randn(*s, dtype=torch.bfloat16) * 0.4)(D, S) * (1.0 / S ** 0.5)),
            "ssm_D": _w((lambda *s: torch.randn(*s, dtype=torch.bfloat16) * 0.2)(D)),
        }
    _A_in = _A_setup()

    def _B_setup():
        B, S, D, F = 1, 2048, 576, 4608
        return {
            "x": _w((lambda *s: torch.randn(*s, dtype=torch.bfloat16) * 0.05)(B, S, D)),
            "w1": _w((lambda *s: torch.randn(*s, dtype=torch.bfloat16) * 0.2)(D, F) * (2.0 / D) ** 0.5),
            "w3": _w((lambda *s: torch.randn(*s, dtype=torch.bfloat16) * 0.2)(D, F) * (2.0 / D) ** 0.5),
            "w2": _w((lambda *s: torch.randn(*s, dtype=torch.bfloat16) * 0.2)(F, D) * (2.0 / F) ** 0.5),
        }
    _B_in = _B_setup()

    return {
        "A_q": _A_in["q"],
        "A_k": _A_in["k"],
        "A_v": _A_in["v"],
        "A_shift_k": _A_in["shift_k"],
        "A_ssm_k": _A_in["ssm_k"],
        "A_ssm_D": _A_in["ssm_D"],
        "B_x": _B_in["x"],
        "B_w1": _B_in["w1"],
        "B_w3": _B_in["w3"],
        "B_w2": _B_in["w2"],
    }

def reference(A_q, A_k, A_v, A_shift_k, A_ssm_k, A_ssm_D, B_x, B_w1, B_w3, B_w2):
    def _A_ref(q, k, v, shift_k, ssm_k, ssm_D):
        B, S, D = q.shape
        ks = shift_k.shape[-1]
        k_pad = _pad(k, ((0, 0), (ks - 1, 0), (0, 0)))
        k_shift = torch.zeros_like(k)
        for t in range(ks):
            k_shift = k_shift + k_pad[:, t:t + S, :] * shift_k[:, ks - 1 - t][None, None, :]
        kv = k_shift * v
        L = 2 * S
        kv_pad = _pad(kv, ((0, 0), (0, S), (0, 0)))
        kf_pad = _pad(ssm_k, ((0, 0), (0, S)))
        kv_f = (lambda x, axes=(-2,-1): torch.fft.rfft2(x.float(), dim=axes))(kv_pad[:, None, :, :], axes=(1, 2))
        kf_f = (lambda x, axes=(-2,-1): torch.fft.rfft2(x.float(), dim=axes))(kf_pad[:, None, :], axes=(1, 2))
        kf_f = (lambda x, *d: x.permute(*d))(kf_f, 1, 2, 0)
        y_f = kv_f * kf_f[None, :, :, :]
        y = (lambda x, s, axes=(-2,-1): torch.fft.irfft2(x, s=s, dim=axes).to(torch.bfloat16))(y_f, (1, L), axes=(1, 2))[:, 0, :S, :]
        y = y + kv * ssm_D[None, None, :]
        return q * y

    def _B_ref(x, w1, w3, w2):
        gate = torch.einsum("bsd,df->bsf", x, w1)
        up = torch.einsum("bsd,df->bsf", x, w3)
        h = _silu(gate) * up
        out = torch.einsum("bsf,fd->bsd", h, w2)
        return out

    x = A_q
    xn = _layernorm(x, axis=-1)
    return x + _A_ref(xn, A_k, A_v, A_shift_k, A_ssm_k, A_ssm_D) + _B_ref(xn, B_w1, B_w3, B_w2)
```

```cpp
#include <hip/hip_runtime.h>
#include <hip/hip_bf16.h>
#include <stdint.h>

namespace nv {
constexpr int S = 2048, D = 576, F = 4608;
typedef unsigned short u16;
__device__ __forceinline__ float b2f(u16 v) { return __uint_as_float(((unsigned)v) << 16); }
__device__ __forceinline__ u16 f2b(float f) { __bf16 b = (__bf16)f; return __builtin_bit_cast(u16, b); }
__device__ __forceinline__ float rb(float f) { return b2f(f2b(f)); }

__global__ void k_ln(const u16* x, u16* xn) {
  int row = blockIdx.x; int lane = threadIdx.x;
  float v[9]; float s = 0;
  for (int i = 0; i < 9; ++i) { v[i] = b2f(x[row * D + lane + 64 * i]); s += v[i]; }
  for (int o = 32; o; o >>= 1) s += __shfl_xor(s, o);
  float m = s / D; float q = 0;
  for (int i = 0; i < 9; ++i) { float d = v[i] - m; q += d * d; }
  for (int o = 32; o; o >>= 1) q += __shfl_xor(q, o);
  float var = rb(q / D); float mb = rb(m);
  float sd = rb(sqrtf(rb(var + 1e-5f)));
  for (int i = 0; i < 9; ++i) xn[row * D + lane + 64 * i] = f2b(rb(v[i] - mb) / sd);
}
__global__ void k_kv(const u16* k, const u16* v, const u16* sk, u16* kv) {
  int idx = blockIdx.x * 256 + threadIdx.x; int t = idx / D, c = idx % D;
  float a = 0;
  for (int j = 0; j < 4; ++j) { int tt = t + j - 3; float kk = tt >= 0 ? b2f(k[tt * D + c]) : 0.f; a = rb(a + rb(kk * b2f(sk[c * 4 + 3 - j]))); }
  kv[idx] = f2b(a * b2f(v[idx]));
}
__global__ void k_conv(const u16* kv, const u16* hk, const u16* dd, u16* y2) {
  __shared__ float u[S]; __shared__ float h[S];
  int c = blockIdx.x;
  for (int t = threadIdx.x; t < S; t += 256) { u[t] = b2f(kv[t * D + c]); h[t] = b2f(hk[c * S + t]); }
  __syncthreads();
  for (int t = threadIdx.x; t < S; t += 256) {
    float a = 0; for (int s = 0; s <= t; ++s) a += h[t - s] * u[s];
    y2[t * D + c] = f2b(rb(a) + rb(u[t] * b2f(dd[c])));
  }
}
__global__ void k_g1(const u16* xn, const u16* w1, const u16* w3, u16* h) {
  __shared__ float xs[16][D];
  int r0 = blockIdx.y * 16; int f = blockIdx.x * 256 + threadIdx.x;
  for (int i = threadIdx.x; i < 16 * D; i += 256) xs[i / D][i % D] = b2f(xn[r0 * D + i]);
  __syncthreads();
  float g[16], u[16]; for (int i = 0; i < 16; ++i) g[i] = u[i] = 0;
  for (int k = 0; k < D; ++k) { float a = b2f(w1[k * F + f]), b = b2f(w3[k * F + f]);
    for (int i = 0; i < 16; ++i) { g[i] += xs[i][k] * a; u[i] += xs[i][k] * b; } }
  for (int i = 0; i < 16; ++i) { float gg = rb(g[i]), uu = rb(u[i]); float sl = rb(gg / (1.f + expf(-gg))); h[(r0 + i) * F + f] = f2b(sl * uu); }
}
__global__ void k_g2(const u16* h, const u16* w2, const u16* x, const u16* xn, const u16* y2, u16* out) {
  __shared__ float hs[8][512];
  int r0 = blockIdx.y * 8; int d = blockIdx.x * 192 + threadIdx.x;
  float a[8]; for (int i = 0; i < 8; ++i) a[i] = 0;
  for (int k0 = 0; k0 < F; k0 += 512) {
    __syncthreads();
    for (int i = threadIdx.x; i < 8 * 512; i += 192) hs[i / 512][i % 512] = b2f(h[(r0 + i / 512) * F + k0 + i % 512]);
    __syncthreads();
    for (int k = 0; k < 512; ++k) { float w = b2f(w2[(k0 + k) * D + d]); for (int i = 0; i < 8; ++i) a[i] += hs[i][k] * w; }
  }
  for (int i = 0; i < 8; ++i) { int o = (r0 + i) * D + d; float A = rb(b2f(xn[o]) * b2f(y2[o])); float r = rb(b2f(x[o]) + A); out[o] = f2b(r + rb(a[i])); }
}
inline void launch(void* const* d_in, void* d_out, void* d_ws, hipStream_t stream) {
  const u16* x = (const u16*)d_in[0]; const u16* k = (const u16*)d_in[1]; const u16* v = (const u16*)d_in[2];
  const u16* sk = (const u16*)d_in[3]; const u16* hk = (const u16*)d_in[4]; const u16* dd = (const u16*)d_in[5];
  const u16* w1 = (const u16*)d_in[7]; const u16* w3 = (const u16*)d_in[8]; const u16* w2 = (const u16*)d_in[9];
  u16* ws = (u16*)d_ws; u16* xn = ws; u16* kv = xn + S * D; u16* y2 = kv + S * D; u16* h = y2 + S * D;
  k_ln<<<S, 64, 0, stream>>>(x, xn);
  k_kv<<<S * D / 256, 256, 0, stream>>>(k, v, sk, kv);
  k_conv<<<D, 256, 0, stream>>>(kv, hk, dd, y2);
  k_g1<<<dim3(F / 256, S / 16), 256, 0, stream>>>(xn, w1, w3, h);
  k_g2<<<dim3(3, S / 8), 192, 0, stream>>>(h, w2, x, xn, y2, (u16*)d_out);
}
}
extern "C" void kernel_launch(void* const* d_in, const int* in_sizes, int n_in, void* d_out, int out_size, void* d_ws, size_t ws_size, hipStream_t stream) {
  nv::launch(d_in, d_out, d_ws, stream);
}
```

```cpp
#include <hip/hip_runtime.h>
#include <stdint.h>

typedef unsigned short u16;
typedef __attribute__((ext_vector_type(8))) __bf16 bf16x8;
typedef __attribute__((ext_vector_type(2))) __bf16 bf16x2;
typedef __attribute__((ext_vector_type(4))) short s16x4;
typedef __attribute__((ext_vector_type(16))) float f32x16;
typedef __attribute__((ext_vector_type(4))) float f32x4;
typedef __attribute__((ext_vector_type(2))) float f32x2;
typedef __attribute__((ext_vector_type(4))) unsigned u32x4;
typedef __attribute__((ext_vector_type(2))) unsigned u32x2;
#define LAS __attribute__((address_space(3)))
#define GAS __attribute__((address_space(1)))
#define DI __device__ __forceinline__

constexpr int S_ = 2048, D_ = 576, F_ = 4608;
constexpr size_t WS_H = 0;
constexpr size_t WS_W2T = WS_H + (size_t)64 * 288 * 1024;
constexpr size_t WS_KVR = WS_W2T + (size_t)18 * 288 * 1024;
constexpr size_t WS_Y2T = WS_KVR + (size_t)D_ * S_ * 2;
constexpr size_t WS_STAT = WS_Y2T + (size_t)D_ * S_ * 2;
constexpr size_t WS_CNT = WS_STAT + (size_t)S_ * 8;

DI float bf_lo(unsigned v) { return __uint_as_float(v << 16); }
DI float bf_hi(unsigned v) { return __uint_as_float(v & 0xffff0000u); }
DI unsigned pk_bf16(float a, float b) { f32x2 f = {a, b}; bf16x2 r = __builtin_convertvector(f, bf16x2); return __builtin_bit_cast(unsigned, r); }
DI float rbf(float a) { return bf_lo(pk_bf16(a, a)); }
#define WAIT_V(n) asm volatile("s_waitcnt vmcnt(" #n ")" ::: "memory")
#define WAIT_L0() asm volatile("s_waitcnt lgkmcnt(0)" ::: "memory")
#define BAR() __builtin_amdgcn_s_barrier()
#define MFMA32(a, b, c) __builtin_amdgcn_mfma_f32_32x32x16_bf16((a), (b), (c), 0, 0, 0)
#define MFMA16(a, b, c) __builtin_amdgcn_mfma_f32_16x16x32_bf16((a), (b), (c), 0, 0, 0)

template <int CTRL> DI float dpp_add(float v) {
  int r = __builtin_amdgcn_update_dpp(0, __float_as_int(v), CTRL, 0xf, 0xf, false);
  return v + __int_as_float(r);
}
DI float row16_sum(float v) {
  v = dpp_add<0x128>(v); v = dpp_add<0x124>(v); v = dpp_add<0x122>(v); v = dpp_add<0x121>(v);
  return v;
}
DI void glds16(const void* g, char* l) {
  unsigned keep;
  unsigned la = __builtin_amdgcn_readfirstlane((unsigned)(uintptr_t)(LAS char*)l);
  asm volatile("s_mov_b32 %0, m0\n\ts_mov_b32 m0, %2\n\ts_nop 0\n\tglobal_load_lds_dwordx4 %1, off\n\ts_mov_b32 m0, %0"
               : "=&s"(keep) : "v"(g), "s"(la) : "memory");
}
DI bf16x8 lds_b128(const char* p) { return *(const bf16x8*)p; }
DI s16x4 lds_tr(const char* p) { return __builtin_amdgcn_ds_read_tr16_b64_v4i16((LAS s16x4*)p); }
DI bf16x8 cat_tr(s16x4 lo, s16x4 hi) {
  auto v = __builtin_shufflevector(lo, hi, 0, 1, 2, 3, 4, 5, 6, 7);
  return __builtin_bit_cast(bf16x8, v);
}

#define STAMP_DECL(kid)
#define STAMP_AT()
constexpr int XT = 36864;
constexpr int WB0 = XT;
constexpr int WCH = 24576;
constexpr int STB = WB0 + 5 * WCH;

__global__ __launch_bounds__(512, 2) void k1(const u16* __restrict__ x, const u16* __restrict__ w1, const u16* __restrict__ w3,
                                             const u16* __restrict__ w2, const u16* __restrict__ kin, const u16* __restrict__ vin,
                                             const u16* __restrict__ sk, char* __restrict__ ws) {
  __shared__ __attribute__((aligned(16))) char smem[163840];
  const int tid = threadIdx.x, lane = tid & 63;
  const int w = __builtin_amdgcn_readfirstlane(tid >> 6);
  const int b = blockIdx.x;
  const int L = (b & 7) * 32 + (b >> 3);
  if (L >= 252) {
    if (L == 252 && tid < 6) *(unsigned*)(ws + WS_CNT + tid * 256) = 0u;
    return;
  }
  const int fg = L / 7, mg = L % 7;
  const int t0 = mg ? 1 + 9 * mg : 0, nt = mg ? 9 : 10;
  const int hh = lane >> 5;
  STAMP_DECL(0) STAMP_AT();

  int xsrc[5];
#pragma unroll
  for (int n = 0; n < 5; ++n) {
    int p = n * 64 + lane; if (p > 287) p = 287;
    int r = p / 72, pos = p % 72, row = 4 * w + r;
    int c = (pos & ~7) | ((pos ^ (row >> 1)) & 7);
    xsrc[n] = row * 1152 + c * 16;
  }
  auto issue_x = [&](int tile, int slot_off) {
    const char* g = (const char*)x + (size_t)tile * XT;
    char* l = smem + slot_off + w * 4608;
#pragma unroll
    for (int n = 0; n < 4; ++n) glds16(g + xsrc[n], l + n * 1024);
    if (lane < 32) glds16(g + xsrc[4], l + 4096);
  };
  const char* wsrc[3];
#pragma unroll
  for (int n = 0; n < 3; ++n) {
    int k = (w * 3 + n) * 2 + hh, mat = (lane >> 4) & 1, pos = lane & 15;
    int ls = (pos >> 1) ^ ((k & 3) | (mat << 2));
    int cc = ls * 2 + (pos & 1);
    wsrc[n] = (const char*)((mat ? w3 : w1) + (size_t)k * F_ + fg * 128 + cc * 8);
  }
  auto issue_w = [&](int c) {
    char* l = smem + WB0 + ((c + 3) % 5) * WCH + w * 3072;
#pragma unroll
    for (int n = 0; n < 3; ++n) glds16(wsrc[n] + (size_t)c * (48 * F_ * 2), l + n * 1024);
  };
  issue_x(t0, 0);
  issue_w(0); issue_w(1); issue_w(2); issue_w(3);

  int xa[4];
  {
    int row = lane & 31, g = (row >> 1) & 7;
#pragma unroll
    for (int v = 0; v < 4; ++v) xa[v] = row * 1152 + (((2 * v + hh) ^ g) << 4);
  }
  int wtr;
  {
    int mat = (lane >> 4) & 1, q = (lane & 15) >> 2, p4 = lane & 3;
    wtr = WB0 + (8 * hh + q) * 512 + mat * 256 + ((w ^ (q | (mat << 2))) * 32) + p4 * 8;
  }
  const int sta = (4 * w + (lane >> 4)) * 1152 + (lane & 15) * 8;

  auto stats = [&](int slot_off, int par, int tile) {
    u32x2 v[9];
#pragma unroll
    for (int i = 0; i < 9; ++i) v[i] = *(const u32x2*)(smem + slot_off + sta + i * 128);
    float f[36];
#pragma unroll
    for (int i = 0; i < 9; ++i) { f[4 * i] = bf_lo(v[i][0]); f[4 * i + 1] = bf_hi(v[i][0]); f[4 * i + 2] = bf_lo(v[i][1]); f[4 * i + 3] = bf_hi(v[i][1]); }
    float s0 = 0.f, s1 = 0.f;
#pragma unroll
    for (int i = 0; i < 18; ++i) { s0 += f[2 * i]; s1 += f[2 * i + 1]; }
    float mean = row16_sum(s0 + s1) * (1.f / 576.f);
    float q0 = 0.f, q1 = 0.f;
#pragma unroll
    for (int i = 0; i < 18; ++i) { float d0 = f[2 * i] - mean, d1 = f[2 * i + 1] - mean; q0 += d0 * d0; q1 += d1 * d1; }
    float var = row16_sum(q0 + q1) * (1.f / 576.f);
    float mb = rbf(mean);
    float sd = rbf(sqrtf(rbf(rbf(var) + 1e-5f)));
    if ((lane & 15) == 0) {
      u32x2 o; o[0] = pk_bf16(-mb, -mb); o[1] = __float_as_uint(1.f / sd);
      *(u32x2*)(smem + STB + par * 256 + (4 * w + (lane >> 4)) * 8) = o;
      if (fg == 0) { f32x2 gsv = {mb, sd}; *(f32x2*)(ws + WS_STAT + (size_t)(tile * 32 + 4 * w + (lane >> 4)) * 8) = gsv; }
    }
  };

  bf16x8 W[36];
  f32x16 acc, cs;
#pragma unroll
  for (int i = 0; i < 16; ++i) { acc[i] = 0.f; cs[i] = 0.f; }
  bf16x8 ones;
#pragma unroll
  for (int i = 0; i < 8; ++i) ones[i] = (__bf16)1.0f;

#pragma unroll
  for (int c = 0; c < 12; ++c) {
    if (c <= 8) WAIT_V(9); else if (c == 9) WAIT_V(6); else if (c == 10) WAIT_V(8); else WAIT_V(10);
    WAIT_L0();
    BAR();
    if (c + 4 < 12) issue_w(c + 4);
    if (c == 9) issue_x(t0 + 1, XT);
    if (c == 10) issue_x(t0 + 2, 2 * XT);
    const char* bp = smem + wtr + ((c + 3) % 5) * WCH;
#pragma unroll
    for (int sl = 0; sl < 3; ++sl) {
      const int s = c * 3 + sl;
      s16x4 lo = lds_tr(bp + sl * 8192), hi = lds_tr(bp + sl * 8192 + 2048);
      W[s] = cat_tr(lo, hi);
      bf16x8 xb = lds_b128(smem + xa[s & 3] + (s >> 2) * 128);
      cs = MFMA32(ones, W[s], cs);
      acc = MFMA32(W[s], xb, acc);
    }
    if (c == 1) stats(0, 0, t0);
    if (c == 0 || c == 5) STAMP_AT();
  }
  STAMP_AT();
  bf16x8 csA;
  {
    float c0 = cs[0];
    float h0 = rbf(c0), r1 = c0 - h0, h1 = rbf(r1), h2 = rbf(r1 - h1);
    u32x4 t; t[0] = hh ? 0u : pk_bf16(h0, h1); t[1] = hh ? 0u : pk_bf16(h2, 0.f); t[2] = 0u; t[3] = 0u;
    csA = __builtin_bit_cast(bf16x8, t);
  }
  char* hdst = ws + WS_H + ((size_t)(fg * 8 + w)) * 1024 + lane * 16;

  auto epilogue = [&](const f32x16& a, int par, int tile) {
    u32x2 st = *(const u32x2*)(smem + STB + par * 256 + (lane & 31) * 8);
    u32x4 mb4 = {st[0], st[0], st[0], st[0]};
    f32x16 e = MFMA32(csA, __builtin_bit_cast(bf16x8, mb4), a);
    float rs = __uint_as_float(st[1]);
    float c1 = rs * -1.4426950408889634f, rs2 = rs * rs;
    float hv[8];
#pragma unroll
    for (int j = 0; j < 8; ++j) {
      float g = e[j], u = e[8 + j];
      float ex = __builtin_amdgcn_exp2f(g * c1);
      float r = __builtin_amdgcn_rcpf(1.f + ex);
      hv[j] = (g * u) * (rs2 * r);
    }
    u32x4 o = {pk_bf16(hv[0], hv[1]), pk_bf16(hv[2], hv[3]), pk_bf16(hv[4], hv[5]), pk_bf16(hv[6], hv[7])};
    *(u32x4*)(hdst + (size_t)tile * (288 * 1024)) = o;
  };

  const int jb = fg * 6 + (mg - 1);
  auto issue_jobA = [&](int slot_off) {
    const int fb = jb / 3, db = jb % 3;
#pragma unroll
    for (int n = 0; n < 3; ++n) {
      int p = (w * 3 + n) * 64 + lane, row = p / 24, ch = p % 24;
      glds16((const char*)(w2 + (size_t)(fb * 64 + row) * D_ + db * 192 + ch * 8), smem + slot_off + (w * 3 + n) * 1024);
    }
  };
  auto issue_jobB = [&](int slot_off) {
    const int tb = jb / 6, cb = jb % 6;
#pragma unroll
    for (int n = 0; n < 4; ++n) {
      int id = w + 8 * n, p = id * 64 + lane;
      if (p < 1584) {
        int row = p / 12, ch = p % 12;
        int t = row < 68 ? tb * 64 - 3 + row : tb * 64 + row - 68;
        t = t < 0 ? 0 : (t > S_ - 1 ? S_ - 1 : t);
        const u16* src = (row < 68 ? kin : vin) + (size_t)t * D_ + cb * 96 + ch * 8;
        glds16((const char*)src, smem + slot_off + id * 1024);
      }
    }
  };

  int cur = XT, nxt2 = 0;
  for (int i = 1; i < nt; ++i) {
    if (i == nt - 1) WAIT_V(2); else if (i == 1) WAIT_V(5); else if (i == 2) WAIT_V(6); else WAIT_V(7);
    WAIT_L0();
    BAR();
    if (i + 2 < nt) issue_x(t0 + i + 2, nxt2);
    else if (mg) { if (i + 2 == nt) issue_jobA(nxt2); else if (jb < 192) issue_jobB(nxt2); }
    epilogue(acc, (i - 1) & 1, t0 + i - 1);
    stats(cur, i & 1, t0 + i);
    f32x16 a2;
#pragma unroll
    for (int r = 0; r < 16; ++r) a2[r] = 0.f;
    const char* xp = smem + cur;
#pragma unroll
    for (int s = 0; s < 36; ++s) {
      bf16x8 xb = lds_b128(xp + xa[s & 3] + (s >> 2) * 128);
      a2 = MFMA32(W[s], xb, a2);
    }
    acc = a2;
    STAMP_AT();
    nxt2 = cur - XT < 0 ? 2 * XT : cur - XT;
    cur = cur + XT > 2 * XT ? 0 : cur + XT;
    nxt2 = cur + 2 * XT; if (nxt2 > 2 * XT) nxt2 -= 3 * XT;
  }
  WAIT_L0();
  BAR();
  epilogue(acc, (nt - 1) & 1, t0 + nt - 1);

  STAMP_AT();
  if (mg == 0) return;
  WAIT_V(0);
  BAR();
  {
    const char* ja = smem + (nt % 3) * XT;
    const int fb = jb / 3, db = jb % 3;
    const int g16 = (lane >> 4) & 1, q = (lane & 15) >> 2, p4 = lane & 3;
#pragma unroll
    for (int n = 0; n < 3; ++n) {
      const int fr = w * 3 + n, uu = fr / 6, dd = fr % 6;
      const char* p = ja + (16 * uu + 4 * hh + q) * 384 + (32 * dd + 16 * g16) * 2 + p4 * 8;
      s16x4 lo = lds_tr(p), hi = lds_tr(p + 8 * 384);
      bf16x8 fv = cat_tr(lo, hi);
      *(bf16x8*)(ws + WS_W2T + ((size_t)((db * 6 + dd) * 288 + fb * 4 + uu)) * 1024 + lane * 16) = fv;
    }
  }
  if (jb < 192 && tid < 384) {
    const char* jbp = smem + ((nt + 1) % 3) * XT;
    const int tb = jb / 6, cb = jb % 6;
    const int cp = tid % 48, seg = tid / 48;
    const int c0 = cb * 96 + cp * 2;
    u32x4 skv = *(const u32x4*)(sk + c0 * 4);
    float sa[4] = {bf_hi(skv[1]), bf_lo(skv[1]), bf_hi(skv[0]), bf_lo(skv[0])};
    float sb[4] = {bf_hi(skv[3]), bf_lo(skv[3]), bf_hi(skv[2]), bf_lo(skv[2])};
    unsigned kr[11], vr[8];
#pragma unroll
    for (int r = 0; r < 11; ++r) kr[r] = *(const unsigned*)(jbp + (seg * 8 + r) * 192 + cp * 4);
#pragma unroll
    for (int r = 0; r < 8; ++r) vr[r] = *(const unsigned*)(jbp + (68 + seg * 8 + r) * 192 + cp * 4);
    if (tb == 0 && seg == 0) { kr[0] = 0u; kr[1] = 0u; kr[2] = 0u; }
    unsigned res[8];
#pragma unroll
    for (int r = 0; r < 8; ++r) {
      unsigned a = pk_bf16(bf_lo(kr[r]) * sa[0], bf_hi(kr[r]) * sb[0]);
#pragma unroll
      for (int j = 1; j < 4; ++j) {
        unsigned pr = pk_bf16(bf_lo(kr[r + j]) * sa[j], bf_hi(kr[r + j]) * sb[j]);
        a = pk_bf16(bf_lo(a) + bf_lo(pr), bf_hi(a) + bf_hi(pr));
      }
      res[r] = pk_bf16(bf_lo(a) * bf_lo(vr[r]), bf_hi(a) * bf_hi(vr[r]));
    }
    u32x4 oa, ob;
#pragma unroll
    for (int d = 0; d < 4; ++d) {
      unsigned e0 = res[7 - 2 * d], e1 = res[6 - 2 * d];
      oa[d] = (e0 & 0xffffu) | (e1 << 16);
      ob[d] = (e0 >> 16) | (e1 & 0xffff0000u);
    }
    const int tt = tb * 64 + seg * 8;
    char* dst = ws + WS_KVR + ((size_t)c0 * S_ + (2040 - tt)) * 2;
    *(u32x4*)dst = oa;
    *(u32x4*)(dst + S_ * 2) = ob;
  }
  STAMP_AT();
}

constexpr int RED_ROW = 96 * 4 + 16;
constexpr int RED_W = 64 * RED_ROW;
constexpr int Y2_OFF = 4 * RED_W;
constexpr int Y2_ROW = 136;
constexpr int CV_COPY = 4160, CV_U = 4 * CV_COPY, CV_W = CV_U + 4608;
constexpr int K2_LDS = Y2_OFF + 96 * Y2_ROW + 64;

__global__ __launch_bounds__(256, 1) void k2(const u16* __restrict__ x, const u16* __restrict__ hk, const u16* __restrict__ dvec,
                                             char* __restrict__ ws, u16* __restrict__ out) {
  __shared__ __attribute__((aligned(16))) char smem[K2_LDS];
  const int tid = threadIdx.x, lane = tid & 63;
  const int w = __builtin_amdgcn_readfirstlane(tid >> 6);
  const int b = blockIdx.x, xcd = b & 7, slot = b >> 3;
  STAMP_DECL(1) STAMP_AT();
  if (slot < 24) {
    const int R = xcd * 4 + slot / 6, Dt = slot % 6;
    const char* hp = ws + WS_H + ((size_t)(2 * R * 288 + w)) * 1024 + lane * 16;
    const char* wp = ws + WS_W2T + ((size_t)(3 * Dt * 288 + w)) * 1024 + lane * 16;
    constexpr int P = 8;
    bf16x8 fa[P][3], fb[P][2];
    f32x16 acc[3][2];
#pragma unroll
    for (int i = 0; i < 3; ++i)
#pragma unroll
      for (int j = 0; j < 2; ++j)
#pragma unroll
        for (int r = 0; r < 16; ++r) acc[i][j][r] = 0.f;
    auto ld = [&](int st, int p) {
#pragma unroll
      for (int j = 0; j < 2; ++j) fb[p][j] = *(const bf16x8*)(hp + (size_t)j * (288 * 1024) + st * 4096);
#pragma unroll
      for (int i = 0; i < 3; ++i) fa[p][i] = *(const bf16x8*)(wp + (size_t)i * (288 * 1024) + st * 4096);
    };
#pragma unroll
    for (int p = 0; p < P; ++p) ld(p, p);
#pragma unroll
    for (int st = 0; st < 72; ++st) {
      const int p = st % P;
#pragma unroll
      for (int i = 0; i < 3; ++i)
#pragma unroll
        for (int j = 0; j < 2; ++j) acc[i][j] = MFMA32(fa[p][i], fb[p][j], acc[i][j]);
      __builtin_amdgcn_sched_barrier(0);
      if (st + P < 72) ld(st + P, p);
      __builtin_amdgcn_sched_barrier(0);
    }
    STAMP_AT();
    {
      char* rp = smem + w * RED_W + (lane & 31) * RED_ROW + (lane >> 5) * 16;
#pragma unroll
      for (int i = 0; i < 3; ++i)
#pragma unroll
        for (int j = 0; j < 2; ++j)
#pragma unroll
          for (int g = 0; g < 4; ++g) {
            f32x4 v = {acc[i][j][4 * g], acc[i][j][4 * g + 1], acc[i][j][4 * g + 2], acc[i][j][4 * g + 3]};
            *(f32x4*)(rp + j * 32 * RED_ROW + (i * 32 + g * 8) * 4) = v;
          }
    }
    {
      GAS unsigned* cnt = (GAS unsigned*)(ws + WS_CNT + Dt * 256);
      for (unsigned spins = 0; spins < (1u << 22); ++spins) {
        unsigned v = __builtin_amdgcn_readfirstlane(__hip_atomic_load(cnt, __ATOMIC_RELAXED, __HIP_MEMORY_SCOPE_AGENT));
        if (v >= 8u) break;
        __builtin_amdgcn_s_sleep(2);
      }
      __builtin_amdgcn_fence(__ATOMIC_ACQUIRE, "wavefront");
      STAMP_AT();
      const __amdgpu_buffer_rsrc_t rs = __builtin_amdgcn_make_buffer_rsrc(ws + WS_Y2T, 0, D_ * S_ * 2, 0x00020000);
#pragma unroll
      for (int n = 0; n < 3; ++n) {
        int ci = n * 256 + tid, d = ci >> 3, ch = ci & 7;
        u32x4 v = __builtin_amdgcn_raw_buffer_load_b128(rs, ((Dt * 96 + d) * S_ + R * 64 + ch * 8) * 2, 0, 16);
        char* yp = smem + Y2_OFF + d * Y2_ROW + ch * 16;
        *(u32x2*)yp = u32x2{v[0], v[1]};
        *(u32x2*)(yp + 8) = u32x2{v[2], v[3]};
      }
    }
    __syncthreads();
#pragma unroll
    for (int n = 0; n < 3; ++n) {
      const int ci = n * 256 + tid, t = ci / 12, c8 = ci % 12;
      const int row = R * 64 + t, col = Dt * 96 + c8 * 8;
      u32x4 xv = *(const u32x4*)(x + (size_t)row * D_ + col);
      f32x2 st = *(const f32x2*)(ws + WS_STAT + (size_t)row * 8);
      f32x4 s0 = {0.f, 0.f, 0.f, 0.f}, s1 = {0.f, 0.f, 0.f, 0.f};
#pragma unroll
      for (int ww = 0; ww < 4; ++ww) {
        const char* rp = smem + ww * RED_W + t * RED_ROW + c8 * 32;
        s0 += *(const f32x4*)rp; s1 += *(const f32x4*)(rp + 16);
      }
      float bsum[8] = {s0[0], s0[1], s0[2], s0[3], s1[0], s1[1], s1[2], s1[3]};
      float o[8];
#pragma unroll
      for (int e = 0; e < 8; ++e) {
        float xf = (e & 1) ? bf_hi(xv[e >> 1]) : bf_lo(xv[e >> 1]);
        float y2 = bf_lo(*(const u16*)(smem + Y2_OFF + (c8 * 8 + e) * Y2_ROW + t * 2));
        float xn = rbf(rbf(xf - st[0]) / st[1]);
        float r = rbf(xf + rbf(xn * y2));
        o[e] = r + rbf(bsum[e]);
      }
      u32x4 ov = {pk_bf16(o[0], o[1]), pk_bf16(o[2], o[3]), pk_bf16(o[4], o[5]), pk_bf16(o[6], o[7])};
      *(u32x4*)(out + (size_t)row * D_ + col) = ov;
    }
    STAMP_AT();
  } else {
    const int cw = xcd * 6 + (slot - 24);
    char* lb = smem + w * CV_W;
    const int a16 = lane & 15, q = lane >> 4;
    const __amdgpu_buffer_rsrc_t yrs = __builtin_amdgcn_make_buffer_rsrc(ws + WS_Y2T, 0, D_ * S_ * 2, 0x00020000);
    for (int rd = 0; rd < 3; ++rd) {
      const int c = cw * 12 + rd * 4 + w;
      {
        const u16* hrow = hk + (size_t)c * S_;
        unsigned G[20];
#pragma unroll
        for (int n = 0; n < 5; ++n) {
          int e0 = 32 * lane - 16 + 8 * n;
          u32x4 v = *(const u32x4*)(hrow + (e0 < 0 ? 0 : e0));
          if (e0 < 0) v = u32x4{0u, 0u, 0u, 0u};
          G[4 * n] = v[0]; G[4 * n + 1] = v[1]; G[4 * n + 2] = v[2]; G[4 * n + 3] = v[3];
        }
        unsigned O[18];
#pragma unroll
        for (int d = 0; d < 18; ++d) O[d] = __builtin_amdgcn_alignbit(G[d + 1], G[d], 16);
#pragma unroll
        for (int n = 0; n < 4; ++n) {
          *(u32x4*)(lb + 0 * CV_COPY + lane * 64 + n * 16) = u32x4{O[4 * n], O[4 * n + 1], O[4 * n + 2], O[4 * n + 3]};
          *(u32x4*)(lb + 1 * CV_COPY + lane * 64 + n * 16) = u32x4{G[4 * n + 1], G[4 * n + 2], G[4 * n + 3], G[4 * n + 4]};
          *(u32x4*)(lb + 2 * CV_COPY + lane * 64 + n * 16) = u32x4{O[4 * n + 1], O[4 * n + 2], O[4 * n + 3], O[4 * n + 4]};
          *(u32x4*)(lb + 3 * CV_COPY + lane * 64 + n * 16) = u32x4{G[4 * n + 2], G[4 * n + 3], G[4 * n + 4], G[4 * n + 5]};
        }
        {
          int r = lane >> 4, i = 2048 + (lane & 15), hi = i + r - 15;
          u16 tv = hrow[hi > S_ - 1 ? S_ - 1 : hi];
          if (hi > S_ - 1) tv = 0;
          *(u16*)(lb + r * CV_COPY + i * 2) = tv;
        }
      }
      {
        const char* ur = ws + WS_KVR + (size_t)c * S_ * 2;
#pragma unroll
        for (int n = 0; n < 4; ++n) *(u32x4*)(lb + CV_U + n * 1024 + lane * 16) = *(const u32x4*)(ur + n * 1024 + lane * 16);
        if (lane < 32) *(u32x4*)(lb + CV_U + 4096 + lane * 16) = u32x4{0u, 0u, 0u, 0u};
      }
      bf16x8 Q[64];
      {
        const char* qp = lb + CV_U + (2032 - 16 * a16 + 8 * q - 32 * 56) * 2;
#pragma unroll
        for (int z = 0; z < 64; ++z) Q[z] = lds_b128(qp + z * 64);
      }
      STAMP_AT();
      f32x4 acc[8];
#pragma unroll
      for (int i = 0; i < 8; ++i) acc[i] = f32x4{0.f, 0.f, 0.f, 0.f};
      {
        const char* pp = lb + (a16 & 3) * CV_COPY + ((a16 & ~3) + 8 * q) * 2;
#pragma unroll
        for (int s = 0; s < 64; ++s) {
          u32x2 lo = *(const u32x2*)(pp + s * 64), hi = *(const u32x2*)(pp + s * 64 + 8);
          u32x4 pv = {lo[0], lo[1], hi[0], hi[1]};
          bf16x8 Pf = __builtin_bit_cast(bf16x8, pv);
#pragma unroll
          for (int i = s / 8; i < 8; ++i) acc[i] = MFMA16(Pf, Q[s - 8 * i + 56], acc[i]);
        }
      }
      STAMP_AT();
      {
        const float dv = bf_lo(dvec[c]);
#pragma unroll
        for (int i = 0; i < 8; ++i) {
          const int tq = 256 * i + 16 * a16 + 4 * q;
          u32x2 uv = *(const u32x2*)(lb + CV_U + (2044 - tq) * 2);
          float u0 = bf_hi(uv[1]), u1 = bf_lo(uv[1]), u2 = bf_hi(uv[0]), u3 = bf_lo(uv[0]);
          u32x2 o;
          o[0] = pk_bf16(rbf(acc[i][0]) + rbf(u0 * dv), rbf(acc[i][1]) + rbf(u1 * dv));
          o[1] = pk_bf16(rbf(acc[i][2]) + rbf(u2 * dv), rbf(acc[i][3]) + rbf(u3 * dv));
          __builtin_amdgcn_raw_buffer_store_b64(o, yrs, (c * S_ + tq) * 2, 0, 16);
        }
      }
    }
    WAIT_V(0);
    __syncthreads();
    STAMP_AT();
    if (tid == 0) __hip_atomic_fetch_add((GAS unsigned*)(ws + WS_CNT + (cw / 8) * 256), 1u, __ATOMIC_RELAXED, __HIP_MEMORY_SCOPE_AGENT);
  }
}

extern "C" void kernel_launch(void* const* d_in, const int* in_sizes, int n_in, void* d_out, int out_size, void* d_ws, size_t ws_size,
                              hipStream_t stream) {
  const u16* x = (const u16*)d_in[0];
  const u16* kin = (const u16*)d_in[1];
  const u16* vin = (const u16*)d_in[2];
  const u16* sk = (const u16*)d_in[3];
  const u16* hk = (const u16*)d_in[4];
  const u16* dv = (const u16*)d_in[5];
  const u16* w1 = (const u16*)d_in[7];
  const u16* w3 = (const u16*)d_in[8];
  const u16* w2 = (const u16*)d_in[9];
  k1<<<256, 512, 0, stream>>>(x, w1, w3, w2, kin, vin, sk, (char*)d_ws);
  k2<<<240, 256, 0, stream>>>(x, hk, dv, (char*)d_ws, (u16*)d_out);
}
```

```cpp
#include <hip/hip_runtime.h>
#include <stdint.h>

typedef unsigned short u16;
typedef __attribute__((ext_vector_type(8))) __bf16 bf16x8;
typedef __attribute__((ext_vector_type(2))) __bf16 bf16x2;
typedef __attribute__((ext_vector_type(4))) short s16x4;
typedef __attribute__((ext_vector_type(4))) float f32x4;
typedef __attribute__((ext_vector_type(2))) float f32x2;
typedef __attribute__((ext_vector_type(4))) unsigned u32x4;
typedef __attribute__((ext_vector_type(2))) unsigned u32x2;
#define LAS __attribute__((address_space(3)))
#define GAS __attribute__((address_space(1)))
#define DI __device__ __forceinline__

constexpr int S_ = 2048, D_ = 576, F_ = 4608;
constexpr size_t WS_H = 0;
constexpr size_t WS_W2T = WS_H + (size_t)128 * 144 * 1024;
constexpr size_t WS_KVR = WS_W2T + (size_t)36 * 144 * 1024;
constexpr size_t WS_Y2T = WS_KVR + (size_t)D_ * S_ * 2;
constexpr size_t WS_STAT = WS_Y2T + (size_t)D_ * S_ * 2;
constexpr size_t WS_CNT = WS_STAT + (size_t)S_ * 8;
constexpr size_t WS_EPOCH = WS_CNT + 64 * 256;
constexpr size_t WS_PART = WS_EPOCH + 4096;

#define HAUX 16
#define WNT 1
#define GATH 15
#define K1B 1
#define K1D 3
#define PBF16 1
#define G2P0 7
#define G2P1 7
#define G2ROT 0
#define G2B 1
#define G2D 2
#define G2ORD(i) ((((i) & 1) << 2) | ((i) >> 1))
#define G2S0 7
#define G2S1 7
#define XNT 0
#define K1PF 3
#define G2P 6

DI float bf_lo(unsigned v) { return __uint_as_float(v << 16); }
DI float bf_hi(unsigned v) { return __uint_as_float(v & 0xffff0000u); }
DI unsigned pk_bf16(float a, float b) { f32x2 f = {a, b}; bf16x2 r = __builtin_convertvector(f, bf16x2); return __builtin_bit_cast(unsigned, r); }
DI float rbf(float a) { return bf_lo(pk_bf16(a, a)); }
#define WAIT_V(n) asm volatile("s_waitcnt vmcnt(" #n ")" ::: "memory")
#define WAIT_L0() asm volatile("s_waitcnt lgkmcnt(0)" ::: "memory")
#define BAR() do { __builtin_amdgcn_s_barrier(); asm volatile("" ::: "memory"); } while (0)
#define SB() __builtin_amdgcn_sched_barrier(0)
#define PIN(x) asm volatile("" : "+v"(x))
#define MFMA16(a, b, c) __builtin_amdgcn_mfma_f32_16x16x32_bf16((a), (b), (c), 0, 0, 0)

template <int N> struct IC { static constexpr int value = N; };
template <int I, int N, class Fn> DI void static_for(Fn&& f) {
  if constexpr (I < N) { f(IC<I>{}); static_for<I + 1, N>(f); }
}
template <int CTRL> DI float dpp_add(float v) {
  int r = __builtin_amdgcn_update_dpp(0, __float_as_int(v), CTRL, 0xf, 0xf, false);
  return v + __int_as_float(r);
}
DI float row16_sum(float v) {
  v = dpp_add<0x128>(v); v = dpp_add<0x124>(v); v = dpp_add<0x122>(v); v = dpp_add<0x121>(v);
  return v;
}
template <int NT> DI void glds16(const void* g, char* l) {
  unsigned keep;
  unsigned la = __builtin_amdgcn_readfirstlane((unsigned)(uintptr_t)(LAS char*)l);
  if (NT)
    asm volatile("s_mov_b32 %0, m0\n\ts_mov_b32 m0, %2\n\ts_nop 0\n\tglobal_load_lds_dwordx4 %1, off nt\n\ts_mov_b32 m0, %0"
                 : "=&s"(keep) : "v"(g), "s"(la) : "memory");
  else
    asm volatile("s_mov_b32 %0, m0\n\ts_mov_b32 m0, %2\n\ts_nop 0\n\tglobal_load_lds_dwordx4 %1, off\n\ts_mov_b32 m0, %0"
                 : "=&s"(keep) : "v"(g), "s"(la) : "memory");
}
template <int NT> DI void glds16s(const void* sbase, int voff, char* l) {
  unsigned keep;
  unsigned la = __builtin_amdgcn_readfirstlane((unsigned)(uintptr_t)(LAS char*)l);
  if (NT)
    asm volatile("s_mov_b32 %0, m0\n\ts_mov_b32 m0, %3\n\ts_nop 2\n\tglobal_load_lds_dwordx4 %1, %2 nt\n\ts_mov_b32 m0, %0"
                 : "=&s"(keep) : "v"(voff), "s"(sbase), "s"(la) : "memory");
  else
    asm volatile("s_mov_b32 %0, m0\n\ts_mov_b32 m0, %3\n\ts_nop 2\n\tglobal_load_lds_dwordx4 %1, %2\n\ts_mov_b32 m0, %0"
                 : "=&s"(keep) : "v"(voff), "s"(sbase), "s"(la) : "memory");
}
template <int NT> DI void glds16a(const void* sbase, int voff, unsigned la) {
  unsigned keep;
  if (NT)
    asm volatile("s_mov_b32 %0, m0\n\ts_mov_b32 m0, %3\n\ts_nop 0\n\tglobal_load_lds_dwordx4 %1, %2 nt\n\ts_mov_b32 m0, %0"
                 : "=&s"(keep) : "v"(voff), "s"(sbase), "s"(la) : "memory");
  else
    asm volatile("s_mov_b32 %0, m0\n\ts_mov_b32 m0, %3\n\ts_nop 0\n\tglobal_load_lds_dwordx4 %1, %2\n\ts_mov_b32 m0, %0"
                 : "=&s"(keep) : "v"(voff), "s"(sbase), "s"(la) : "memory");
}
DI bf16x8 lds_b128(const char* p) { return *(const bf16x8*)p; }
DI u32x2 lds_b64v(const char* p) {
  unsigned long long v = *(const volatile LAS unsigned long long*)(LAS const char*)p;
  return u32x2{(unsigned)v, (unsigned)(v >> 32)};
}
DI s16x4 lds_tr(const char* p) { return __builtin_amdgcn_ds_read_tr16_b64_v4i16((LAS s16x4*)p); }
DI bf16x8 cat_tr(s16x4 lo, s16x4 hi) {
  auto v = __builtin_shufflevector(lo, hi, 0, 1, 2, 3, 4, 5, 6, 7);
  return __builtin_bit_cast(bf16x8, v);
}

#define STAMP_DECL(kid)
#define STAMP_AT()

constexpr int XTB = 36864;
constexpr int XT = XTB + 512;
constexpr int WB0 = 2 * XT;
constexpr int WCH = 16384;
constexpr int STB = WB0 + 5 * WCH;

struct Acc { f32x4 v[2][2]; };

typedef GAS unsigned long long gu64;
#define RLX_AGENT __ATOMIC_RELAXED, __HIP_MEMORY_SCOPE_AGENT

__global__ __launch_bounds__(512, 2) void k1(const u16* __restrict__ x, const u16* __restrict__ w1, const u16* __restrict__ w3,
                                             const u16* __restrict__ w2, const u16* __restrict__ kin, const u16* __restrict__ vin,
                                             const u16* __restrict__ sk, char* __restrict__ ws) {
  __shared__ __attribute__((aligned(16))) char smem[STB + 5120];
  const int tid = threadIdx.x, lane = tid & 63;
  const int w = __builtin_amdgcn_readfirstlane(tid >> 6);
  const int b = blockIdx.x;
  const int L = (b & 7) * 32 + (b >> 3);
  const bool gemm = L < 252;
  const int fg = L / 7, mg = L % 7;
  const int t0 = mg ? 1 + 9 * mg : 0, nt = mg ? 9 : 10;
  const int r16 = lane & 15, q = lane >> 4;
  const int rot = (mg * 18) / 7;
  STAMP_DECL(0) STAMP_AT();

  gu64* const gran = (gu64*)(ws + WS_STAT);
  unsigned tag = *(const __attribute__((address_space(4))) unsigned*)(ws + WS_EPOCH) + 1u;
  asm volatile("" : "+s"(tag));
  unsigned xr[5];
  auto row_loads = [&]() {
    const unsigned* xrow = (const unsigned*)(x + (size_t)(b * 8 + w) * D_);
    const unsigned* p4 = xrow + (lane < 32 ? 256 + lane : lane);
    const unsigned* p0 = xrow + lane;
    asm volatile("global_load_dword %0, %5, off\n\tglobal_load_dword %1, %5, off offset:256\n\t"
                 "global_load_dword %2, %5, off offset:512\n\tglobal_load_dword %3, %5, off offset:768\n\tglobal_load_dword %4, %6, off"
                 : "=&v"(xr[0]), "=&v"(xr[1]), "=&v"(xr[2]), "=&v"(xr[3]), "=&v"(xr[4])
                 : "v"(p0), "v"(p4) : "memory");
  };

  const char* wsrc[2];
#pragma unroll
  for (int n = 0; n < 2; ++n) {
    int k = (w * 2 + n) * 2 + (lane >> 5), mat = (lane >> 4) & 1, pos = lane & 15;
    int ls = (pos >> 1) ^ ((k & 3) | (((k >> 3) & 1) << 2));
    int cc = ls * 2 + (pos & 1);
    wsrc[n] = (const char*)((mat ? w3 : w1) + (size_t)k * F_ + fg * 128 + cc * 8);
  }
  int xsrc[5];
  auto make_xsrc = [&]() {
#pragma unroll
    for (int n = 0; n < 5; ++n) {
      const int r = n == 0 ? 0 : n - 1 + (lane >= 8 * n ? 1 : 0);
      const int pos = n * 64 + lane - 72 * r;
      int row = 4 * w + r; if (row > 31) row = 31;
      int c = pos ^ ((row >> 1) & 7);
      c += 4 * rot; if (c >= 72) c -= 72;
      xsrc[n] = row * 1152 + c * 16;
    }
  };
  auto issue_x = [&](int tile, int slot_off) {
    const char* g = (const char*)x + (size_t)tile * XTB;
    const unsigned l = (unsigned)(uintptr_t)(LAS char*)smem + slot_off + w * 4608;
#pragma unroll
    for (int n = 0; n < 5; ++n) glds16a<XNT>(g, xsrc[n], l + n * 1024);
  };
  const unsigned lds0 = (unsigned)(uintptr_t)(LAS char*)smem;
  auto issue_wp = [&](int c, int n) {
    char* l = smem + WB0 + ((c + 2) % 5) * WCH + w * 2048;
    int ca = c + rot; if (ca >= 18) ca -= 18;
    glds16<WNT>(wsrc[n] + (size_t)ca * (32 * F_ * 2), l + n * 1024);
  };
  auto issue_w = [&](int c) { issue_wp(c, 0); issue_wp(c, 1); };
  auto row_stats = [&]() {
    asm volatile("" : "+v"(xr[0]), "+v"(xr[1]), "+v"(xr[2]), "+v"(xr[3]), "+v"(xr[4]) :: "memory");
    float f[10];
#pragma unroll
    for (int i = 0; i < 5; ++i) { f[2 * i] = bf_lo(xr[i]); f[2 * i + 1] = bf_hi(xr[i]); }
    const bool tail = lane < 32;
    float s = 0.f;
#pragma unroll
    for (int i = 0; i < 8; ++i) s += f[i];
    s += tail ? f[8] + f[9] : 0.f;
    s = row16_sum(s); s += __shfl_xor(s, 16); s += __shfl_xor(s, 32);
    const float mean = s * (1.f / 576.f);
    float qq = 0.f;
#pragma unroll
    for (int i = 0; i < 8; ++i) { float d = f[i] - mean; qq += d * d; }
    { float d0 = f[8] - mean, d1 = f[9] - mean; qq += tail ? d0 * d0 + d1 * d1 : 0.f; }
    qq = row16_sum(qq); qq += __shfl_xor(qq, 16); qq += __shfl_xor(qq, 32);
    const float sd = sqrtf(rbf(rbf(qq * (1.f / 576.f)) + 1e-5f));
    if (lane == 0) __hip_atomic_store(gran + b * 8 + w, ((unsigned long long)tag << 32) | pk_bf16(mean, sd), RLX_AGENT);
  };
  if (gemm) { issue_w(0); SB(); make_xsrc(); issue_x(t0, 0); issue_w(1); issue_w(2); }
  row_loads();
  if (!gemm) {
    WAIT_V(0);
    row_stats();
    if (L == 252 && tid < 64) *(unsigned*)(ws + WS_CNT + tid * 256) = 0u;
    if (L == 255 && w == 0) {
      for (unsigned spins = 0; spins < (1u << 20); ++spins) {
        bool ok = true;
#pragma unroll 8
        for (int i = 0; i < 32; ++i) ok &= (unsigned)(__hip_atomic_load(gran + i * 64 + lane, RLX_AGENT) >> 32) == tag;
        if (__all(ok)) break;
        __builtin_amdgcn_s_sleep(8);
      }
      if (lane == 0) __hip_atomic_store((GAS unsigned*)(ws + WS_EPOCH), tag, RLX_AGENT);
    }
    return;
  }
  STAMP_AT();

  int xa[4];
  {
    int g = (r16 >> 1) & 7;
#pragma unroll
    for (int v = 0; v < 4; ++v) xa[v] = (r16 + 16 * (v >> 1)) * 1152 + (((4 * (v & 1) + q) ^ g) << 4);
  }
#define XADDR(n) (xa[((n) & 1) * 2 + (((n) >> 1) & 1)] + ((n) >> 2) * 128)
  int wtr;
  {
    int rr = (lane & 15) >> 2, p4 = lane & 3;
    wtr = WB0 + (8 * q + rr) * 512 + ((w ^ (rr | ((q & 1) << 2))) * 32) + p4 * 8;
  }

  bf16x8 W[18][2];
  Acc accA, accB;
  f32x4 cs[2];
#pragma unroll
  for (int i = 0; i < 2; ++i) { cs[i] = f32x4{0.f, 0.f, 0.f, 0.f}; accA.v[0][i] = cs[i]; accA.v[1][i] = cs[i]; }
  bf16x8 ones;
#pragma unroll
  for (int i = 0; i < 8; ++i) ones[i] = (__bf16)1.0f;

  auto gather_stats = [&]() {
    if (w < 5) {
      const int r = tid < nt * 32 ? tid : 0;
      unsigned long long gv;
      for (unsigned spins = 0; spins < (1u << 20); ++spins) {
        gv = __hip_atomic_load(gran + t0 * 32 + r, RLX_AGENT);
        if (__all((unsigned)(gv >> 32) == tag)) break;
        __builtin_amdgcn_s_sleep(4);
      }
      const float mb = bf_lo((unsigned)gv), sd = bf_hi((unsigned)gv);
      f32x4 o = {-mb, -1.4426950408889634f * __builtin_amdgcn_rcpf(sd), sd * sd, 0.f};
      *(f32x4*)(smem + STB + tid * 16) = o;
    }
  };
  constexpr int LAG = 4;
#pragma unroll
  for (int i = 0; i < 2; ++i) { accB.v[0][i] = cs[0]; accB.v[1][i] = cs[0]; }
  bf16x8 xp_[4];
  static_for<0, 19>([&](auto cc) {
    constexpr int c = decltype(cc)::value;
    constexpr int sa = c - 1, sb = c - 1 - LAG;
    if constexpr (c < 18) {
      if constexpr (c == 0) WAIT_V(9);
      else if constexpr (c <= 2) WAIT_V(14);
      else if constexpr (c == 3) WAIT_V(9);
      else if constexpr (c <= 15) WAIT_V(4);
      else if constexpr (c == 16) WAIT_V(2); else WAIT_V(0);
      BAR();
    }
    SB();
    const char* bp = smem + wtr + ((c + 2) % 5) * WCH;
    s16x4 a0, a1, b0, b1;
    if constexpr (sa >= 0) { accA.v[0][0] = MFMA16(W[sa][0], xp_[0], accA.v[0][0]); accA.v[0][1] = MFMA16(W[sa][1], xp_[0], accA.v[0][1]); }
    if constexpr (c < 18) { a0 = lds_tr(bp); a1 = lds_tr(bp + 2048); xp_[0] = lds_b128(smem + XADDR(2 * c)); }
    SB();
    if constexpr (sa >= 0) { accA.v[1][0] = MFMA16(W[sa][0], xp_[1], accA.v[1][0]); accA.v[1][1] = MFMA16(W[sa][1], xp_[1], accA.v[1][1]); }
    if constexpr (c < 18) { b0 = lds_tr(bp + 256); b1 = lds_tr(bp + 2048 + 256); xp_[1] = lds_b128(smem + XADDR(2 * c + 1)); }
    SB();
    if constexpr (sb >= 0) { accB.v[0][0] = MFMA16(W[sb][0], xp_[2], accB.v[0][0]); accB.v[0][1] = MFMA16(W[sb][1], xp_[2], accB.v[0][1]); }
    if constexpr (sb + 1 >= 0) xp_[2] = lds_b128(smem + XT + XADDR(2 * (sb + 1)));
    if constexpr (c + 3 < 18) issue_wp(c + 3, 0);
    SB();
    if constexpr (sb >= 0) { accB.v[1][0] = MFMA16(W[sb][0], xp_[3], accB.v[1][0]); accB.v[1][1] = MFMA16(W[sb][1], xp_[3], accB.v[1][1]); }
    if constexpr (sb + 1 >= 0) xp_[3] = lds_b128(smem + XT + XADDR(2 * (sb + 1) + 1));
    if constexpr (c + 3 < 18) issue_wp(c + 3, 1);
    SB();
    if constexpr (sa >= 0) { cs[0] = MFMA16(W[sa][0], ones, cs[0]); cs[1] = MFMA16(W[sa][1], ones, cs[1]); }
    if constexpr (c == 0) issue_x(t0 + 1, XT);
    if constexpr (c == 17) issue_x(t0 + 2, 2 * XT);
    if constexpr (c < 18) { W[c][0] = cat_tr(a0, a1); W[c][1] = cat_tr(b0, b1); }
    SB();
    if constexpr (c == 3) row_stats();
    if constexpr (c == GATH) gather_stats();
    if constexpr (c == 0 || c == 3 || c == 8) STAMP_AT();
  });
  STAMP_AT();
  WAIT_L0();
  BAR();
  issue_x(t0 + 3, 3 * XT);
  STAMP_AT();

  const __amdgpu_buffer_rsrc_t hrs = __builtin_amdgcn_make_buffer_rsrc(ws + WS_H, 0, 128 * 144 * 1024, 0x00020000);
  const int hoff = (((q & 1) * 144 + ((fg * 8 + w) >> 1)) * 64 + (2 * (w & 1) + (q >> 1)) * 16 + r16) * 16;
  auto h_store = [&](const float (&hv)[8], int tile) {
    unsigned o0 = pk_bf16(hv[0], hv[1]), o1 = pk_bf16(hv[2], hv[3]), p0 = pk_bf16(hv[4], hv[5]), p1 = pk_bf16(hv[6], hv[7]);
    auto s0 = __builtin_amdgcn_permlane16_swap(o0, p0, false, false);
    auto s1 = __builtin_amdgcn_permlane16_swap(o1, p1, false, false);
    u32x4 o = {s0[0], s1[0], s0[1], s1[1]};
    __builtin_amdgcn_raw_buffer_store_b128(o, hrs, hoff + tile * (2 * 144 * 1024), 0, HAUX);
  };

  const int jb = fg * 6 + (mg - 1);
  auto issue_jobA = [&](int slot_off) {
    const int fb = jb / 3, db = jb % 3;
    int ln = lane; asm volatile("" : "+v"(ln));
#pragma unroll
    for (int n = 0; n < 3; ++n) {
      int p = (w * 3 + n) * 64 + ln, row = p / 24, ch = p % 24;
      glds16<1>((const char*)(w2 + (size_t)(fb * 64 + row) * D_ + db * 192 + ch * 8), smem + slot_off + (w * 3 + n) * 1024);
    }
  };
  auto issue_jobB = [&](int slot_off) {
    const int tb = jb / 6, cb = jb % 6;
    int ln = lane; asm volatile("" : "+v"(ln));
#pragma unroll
    for (int n = 0; n < 4; ++n) {
      int id = w + 8 * n, p = id * 64 + ln;
      if (p < 1584) {
        int row = p / 12, ch = p % 12;
        int t = row < 68 ? tb * 64 - 3 + row : tb * 64 + row - 68;
        t = t < 0 ? 0 : (t > S_ - 1 ? S_ - 1 : t);
        const u16* src = (row < 68 ? kin : vin) + (size_t)t * D_ + cb * 96 + ch * 8;
        glds16<1>((const char*)src, smem + slot_off + id * 1024);
      }
    }
  };

  constexpr int PF = K1PF;
  int sto = STB + r16 * 16;
  {
    const f32x4 st0 = *(const f32x4*)(smem + sto), st1 = *(const f32x4*)(smem + sto + 256);
    sto += 512;
    float hv[8];
    static_for<0, 2 * LAG>([&](auto kc) {
      constexpr int k = decltype(kc)::value, sb = 18 - LAG + (k >> 1), hf = k & 1;
      accB.v[hf][0] = MFMA16(W[sb][0], xp_[2 + hf], accB.v[hf][0]); accB.v[hf][1] = MFMA16(W[sb][1], xp_[2 + hf], accB.v[hf][1]);
      if constexpr (sb + 1 < 18) xp_[2 + hf] = lds_b128(smem + XT + XADDR(2 * (sb + 1) + hf));
      constexpr int h2 = k >> 2, r = k & 3;
      const f32x4& st = h2 ? st1 : st0;
      const float g = __builtin_fmaf(st[0], cs[0][r], accA.v[h2][0][r]), u = __builtin_fmaf(st[0], cs[1][r], accA.v[h2][1][r]);
      const float e = __builtin_amdgcn_exp2f(g * st[1]);
      hv[k] = (g * u) * __builtin_amdgcn_rcpf(__builtin_fmaf(e, st[2], st[2])); PIN(hv[k]);
      SB();
    });
    WAIT_V(5);
    BAR();
    h_store(hv, t0);
  }
  int cur = 2 * XT;
#pragma unroll
  for (int v = 0; v < 4; ++v) xa[v] += 2 * XT;
  bf16x8 xh[PF];
#pragma unroll
  for (int n = 0; n < PF; ++n) xh[n] = lds_b128(smem + XADDR(n));
  STAMP_AT();
  auto body = [&](int i, const Acc& ap, Acc& an) {
    bf16x8 xb[36 + PF];
#pragma unroll
    for (int n = 0; n < PF; ++n) xb[n] = xh[n];
    const f32x4 st0 = *(const f32x4*)(smem + sto), st1 = *(const f32x4*)(smem + sto + 256);
    sto += 512;
    float gg[8], ex[8], pp[8], hv[8];
    const f32x4 zero4 = {0.f, 0.f, 0.f, 0.f};
    const int dst = cur >= 2 * XT ? cur - 2 * XT : cur + 2 * XT;
    const int dl = cur == 3 * XT ? -3 * XT : XT;
    const bool do_x = i + 2 < nt;
    const char* xg = (const char*)x + (size_t)(t0 + (do_x ? i + 2 : nt - 1)) * XTB;
    const int prv = cur == 0 ? 3 * XT : cur - XT;
    const unsigned xl = lds0 + dst + w * 4608;
    SB();
    static_for<0, 36>([&](auto nc) {
      constexpr int n = decltype(nc)::value;
      constexpr int ks = n >> 1, hf = n & 1;
      an.v[hf][0] = MFMA16(W[ks][0], xb[n], ks ? an.v[hf][0] : zero4);
      an.v[hf][1] = MFMA16(W[ks][1], xb[n], ks ? an.v[hf][1] : zero4);
      if constexpr (n + PF == 36) {
#pragma unroll
        for (int v = 0; v < 4; ++v) { xa[v] += dl; PIN(xa[v]); }
      }
      xb[n + PF] = lds_b128(smem + XADDR((n + PF) % 36));
      {
      if constexpr (n >= 2 && n < 26 && (n - 2) % 3 == 0) {
        constexpr int j = (n - 2) / 3, h2 = j >> 2, r = j & 3;
        const f32x4& st = h2 ? st1 : st0;
        gg[j] = __builtin_fmaf(st[0], cs[0][r], ap.v[h2][0][r]); PIN(gg[j]);
        pp[j] = __builtin_fmaf(st[0], cs[1][r], ap.v[h2][1][r]); PIN(pp[j]);
        ex[j] = gg[j] * st[1]; PIN(ex[j]);
      }
      if constexpr (n >= 3 && n < 27 && (n - 3) % 3 == 0) {
        constexpr int j = (n - 3) / 3;
        ex[j] = __builtin_amdgcn_exp2f(ex[j]); PIN(ex[j]);
        pp[j] = gg[j] * pp[j]; PIN(pp[j]);
      }
      if constexpr (n >= 4 && n < 28 && (n - 4) % 3 == 0) {
        constexpr int j = (n - 4) / 3;
        const float k2 = (j >> 2) ? st1[2] : st0[2];
        ex[j] = __builtin_fmaf(ex[j], k2, k2); PIN(ex[j]);
        ex[j] = __builtin_amdgcn_rcpf(ex[j]); PIN(ex[j]);
      }
      if constexpr (n >= 6 && n < 30 && (n - 6) % 3 == 0) { constexpr int j = (n - 6) / 3; hv[j] = pp[j] * ex[j]; PIN(hv[j]); }
      }
      if constexpr (n == 29) h_store(hv, t0 + i - 1);
      if constexpr (n == 3) {
        if (do_x) {
#pragma unroll
          for (int k = 0; k < 5; ++k) glds16a<XNT>(xg, xsrc[k], xl + k * 1024);
        }
      }
      if constexpr (n == 27) { if (!do_x && mg) { if (i + 2 == nt) issue_jobA(dst); else if (jb < 192) issue_jobB(dst); } }
      if constexpr (n == 30) {
        if (do_x) WAIT_V(7); else if (i + 2 == nt) { if (mg) WAIT_V(5); else WAIT_V(2); }
        BAR();
      }
      SB();
    });
#pragma unroll
    for (int n = 0; n < PF; ++n) xh[n] = xb[36 + n];
    cur += dl;
  };
  auto plain_epilogue = [&](const Acc& ap, int tile) {
    const f32x4 st0 = *(const f32x4*)(smem + sto), st1 = *(const f32x4*)(smem + sto + 256);
    sto += 512;
    float hv[8];
#pragma unroll
    for (int j = 0; j < 8; ++j) {
      const int h2 = j >> 2, r = j & 3;
      const f32x4& st = h2 ? st1 : st0;
      const float g = __builtin_fmaf(st[0], cs[0][r], ap.v[h2][0][r]), u = __builtin_fmaf(st[0], cs[1][r], ap.v[h2][1][r]);
      const float e = __builtin_amdgcn_exp2f(g * st[1]);
      hv[j] = (g * u) * __builtin_amdgcn_rcpf(__builtin_fmaf(e, st[2], st[2]));
    }
    h_store(hv, tile);
  };
  for (int i = 2;; i += 2) {
    body(i, accB, accA);
    if (i + 1 >= nt) { plain_epilogue(accA, t0 + nt - 1); break; }
    body(i + 1, accA, accB);
    if (i + 2 >= nt) { plain_epilogue(accB, t0 + nt - 1); break; }
  }
  STAMP_AT();
  if (mg == 0) return;
  WAIT_V(0);
  BAR();
  {
    const char* ja = smem + (nt % 4) * XT;
    const int fb = jb / 3, db = jb % 3;
    const int rr = (lane & 15) >> 2, p4 = lane & 3;
#pragma unroll
    for (int n = 0; n < 3; ++n) {
      const int fr = w * 3 + n, kb = fr / 12, dd = fr % 12;
      const char* p = ja + (32 * kb + 8 * q + rr) * 384 + dd * 32 + p4 * 8;
      s16x4 lo = lds_tr(p), hi = lds_tr(p + 4 * 384);
      bf16x8 fv = cat_tr(lo, hi);
      *(bf16x8*)(ws + WS_W2T + ((size_t)((db * 12 + dd) * 144 + fb * 2 + kb)) * 1024 + lane * 16) = fv;
    }
  }
  if (jb < 192 && tid < 384) {
    const char* jbp = smem + ((nt + 1) % 4) * XT;
    const int tb = jb / 6, cb = jb % 6;
    const int cp = tid % 48, seg = tid / 48;
    const int c0 = cb * 96 + cp * 2;
    u32x4 skv = *(const u32x4*)(sk + c0 * 4);
    float sa[4] = {bf_hi(skv[1]), bf_lo(skv[1]), bf_hi(skv[0]), bf_lo(skv[0])};
    float sb[4] = {bf_hi(skv[3]), bf_lo(skv[3]), bf_hi(skv[2]), bf_lo(skv[2])};
    unsigned kr[11], vr[8];
#pragma unroll
    for (int r = 0; r < 11; ++r) kr[r] = *(const unsigned*)(jbp + (seg * 8 + r) * 192 + cp * 4);
#pragma unroll
    for (int r = 0; r < 8; ++r) vr[r] = *(const unsigned*)(jbp + (68 + seg * 8 + r) * 192 + cp * 4);
    if (tb == 0 && seg == 0) { kr[0] = 0u; kr[1] = 0u; kr[2] = 0u; }
    unsigned res[8];
#pragma unroll
    for (int r = 0; r < 8; ++r) {
      unsigned a = pk_bf16(bf_lo(kr[r]) * sa[0], bf_hi(kr[r]) * sb[0]);
#pragma unroll
      for (int j = 1; j < 4; ++j) {
        unsigned pr = pk_bf16(bf_lo(kr[r + j]) * sa[j], bf_hi(kr[r + j]) * sb[j]);
        a = pk_bf16(bf_lo(a) + bf_lo(pr), bf_hi(a) + bf_hi(pr));
      }
      res[r] = pk_bf16(bf_lo(a) * bf_lo(vr[r]), bf_hi(a) * bf_hi(vr[r]));
    }
    u32x4 oa, ob;
#pragma unroll
    for (int d = 0; d < 4; ++d) {
      unsigned e0 = res[7 - 2 * d], e1 = res[6 - 2 * d];
      oa[d] = (e0 & 0xffffu) | (e1 << 16);
      ob[d] = (e0 >> 16) | (e1 & 0xffff0000u);
    }
    const int tt = tb * 64 + seg * 8;
    char* dst = ws + WS_KVR + ((size_t)c0 * S_ + (2040 - tt)) * 2;
    *(u32x4*)dst = oa;
    *(u32x4*)(dst + S_ * 2) = ob;
  }
  STAMP_AT();
}

constexpr int G_STG = 20480, G_NB = 7;
constexpr int CV_COPY = 4160, CV_U = 2 * CV_COPY, CV_W = CV_U + 4608;
constexpr int G_Y = G_STG * G_NB;
constexpr int G_CNT = G_Y + 12288;
constexpr int K2_LDS = G_CNT + 16;

__global__ __launch_bounds__(512, 2) void k2(const u16* __restrict__ x, const u16* __restrict__ hk, const u16* __restrict__ dvec,
                                             char* __restrict__ ws, u16* __restrict__ out) {
  __shared__ __attribute__((aligned(16))) char smem[K2_LDS];
  const int tid = threadIdx.x, lane = tid & 63;
  const int w = __builtin_amdgcn_readfirstlane(tid >> 6);
  const int b = blockIdx.x, xcd = b & 7, slot = b >> 3;
  const int r16 = lane & 15, q = lane >> 4;
  const __amdgpu_buffer_rsrc_t yrs = __builtin_amdgcn_make_buffer_rsrc(ws + WS_Y2T, 0, D_ * S_ * 2, 0x00020000);
  STAMP_DECL(1) STAMP_AT();
  LAS unsigned* lcnt = (LAS unsigned*)(smem + G_CNT);
  if (tid == 0) *lcnt = 0u;
  if (slot < 24) {
    const int ks = xcd & 3, Mt = (xcd >> 2) * 8 + slot / 3, Dt = slot % 3;
    const int tile = Mt * 3 + Dt;
    const int l16 = lane * 16;
    GAS unsigned* pc = (GAS unsigned*)(ws + WS_CNT + (8 + tile) * 256);
    GAS unsigned* yc = (GAS unsigned*)(ws + WS_CNT + xcd * 256);
    if (w >= 4) {
      const int v = w - 4;
      const unsigned lds0 = (unsigned)(uintptr_t)(LAS char*)smem + v * 1024;
      const char* sh = ws + WS_H + ((size_t)((8 * Mt + v) * 144 + 36 * ks)) * 1024;
      const char* sw = ws + WS_W2T + ((size_t)((12 * Dt + v) * 144 + 36 * ks)) * 1024;
      auto issue = [&](int st, int buf) {
        const unsigned l = lds0 + buf * G_STG;
        glds16a<0>(sh + st * 1024, l16, l);
        glds16a<0>(sh + st * 1024 + 4 * 144 * 1024, l16, l + 4096);
        glds16a<0>(sw + st * 1024, l16, l + 8192);
        glds16a<0>(sw + st * 1024 + 4 * 144 * 1024, l16, l + 12288);
        glds16a<0>(sw + st * 1024 + 8 * 144 * 1024, l16, l + 16384);
      };
#pragma unroll
      for (int st = 0; st < 6; ++st) issue(st, st);
      WAIT_V(25);
      BAR();
      int be = 0;
#pragma unroll 1
      for (int e = 0; e < 36; e += 2) {
        if (e <= 30) WAIT_V(15); else if (e == 32) WAIT_V(5); else WAIT_V(0);
        BAR();
        if (e + 6 < 36) { issue(e + 6, be == 0 ? G_NB - 1 : be - 1); issue(e + 7, be); }
        be = be + 2 >= G_NB ? be + 2 - G_NB : be + 2;
      }
      if (w == 4) {
        for (unsigned spins = 0; spins < (1u << 20); ++spins) {
          if (__builtin_amdgcn_readfirstlane(__hip_atomic_load(yc, RLX_AGENT)) >= 64u) break;
          __builtin_amdgcn_s_sleep(8);
        }
        __builtin_amdgcn_fence(__ATOMIC_ACQUIRE, "wavefront");
        u32x4 yv[12];
        const int yo = ((Dt * 192 + (lane >> 2)) * S_ + Mt * 128 + ((lane >> 1) & 1) * 64 + ks * 16 + (lane & 1) * 8) * 2;
#pragma unroll
        for (int n = 0; n < 12; ++n) yv[n] = __builtin_amdgcn_raw_buffer_load_b128(yrs, yo, n * 16 * S_ * 2, 16);
#pragma unroll
        for (int n = 0; n < 12; ++n) *(u32x4*)(smem + G_Y + n * 1024 + l16) = yv[n];
        WAIT_L0();
      }
      BAR();
      if (w == 5) {
        for (unsigned spins = 0; spins < (1u << 22); ++spins) {
          if (__builtin_amdgcn_readfirstlane(__hip_atomic_load(pc, RLX_AGENT)) >= 4u) break;
          __builtin_amdgcn_s_sleep(1);
        }
      }
      BAR();
      return;
    }
    const int wm = w >> 1, wd = w & 1;
    f32x4 acc[6][4];
#pragma unroll
    for (int i = 0; i < 6; ++i)
#pragma unroll
      for (int j = 0; j < 4; ++j) acc[i][j] = f32x4{0.f, 0.f, 0.f, 0.f};
    const int ra = (8 + 6 * wd) * 1024 + l16, rb = 4 * wm * 1024 + l16;
    bf16x8 fa[2][6], fb[2][4];
    const int row = Mt * 128 + wm * 64 + ks * 16 + r16, col = Dt * 192 + wd * 96 + 4 * q;
    u32x2 xv[6];
#pragma unroll
    for (int i = 0; i < 6; ++i) xv[i] = __builtin_nontemporal_load((const u32x2*)(x + (size_t)row * D_ + col + 16 * i));
    const unsigned stw = *(const unsigned*)(ws + WS_STAT + (size_t)row * 8);
    BAR();
#pragma unroll
    for (int j = 0; j < 4; ++j) fb[0][j] = lds_b128(smem + rb + j * 1024);
#pragma unroll
    for (int i = 0; i < 6; ++i) fa[0][i] = lds_b128(smem + ra + i * 1024);
    STAMP_AT();
    int be = 0;
#pragma unroll 1
    for (int e = 0; e < 36; e += 2) {
      WAIT_L0();
      BAR();
      const int b1 = be + 1 >= G_NB ? be + 1 - G_NB : be + 1, b2 = be + 2 >= G_NB ? be + 2 - G_NB : be + 2;
      SB();
      static_for<0, 2>([&](auto pcn) {
        constexpr int p = decltype(pcn)::value;
        const char* la_ = smem + (p ? b2 : b1) * G_STG + ra;
        const char* lb_ = smem + (p ? b2 : b1) * G_STG + rb;
        static_for<0, 24>([&](auto kc) {
          constexpr int k = decltype(kc)::value, i = k >> 2, j = k & 3;
          acc[i][j] = MFMA16(fa[p][i], fb[p][j], acc[i][j]);
          if constexpr ((k & 1) == 0 && k < 8) fb[p ^ 1][k >> 1] = lds_b128(lb_ + (k >> 1) * 1024);
          else if constexpr ((k & 1) == 0 && k < 20) fa[p ^ 1][(k >> 1) - 4] = lds_b128(la_ + ((k >> 1) - 4) * 1024);
          if constexpr (k & 1) SB();
        });
      });
      be = b2;
    }
    STAMP_AT();
    const __amdgpu_buffer_rsrc_t prs = __builtin_amdgcn_make_buffer_rsrc(ws + WS_PART, 0, 48 * 16 * 24 * 1024, 0x00020000);
#pragma unroll
    for (int j = 0; j < 4; ++j) {
      if (j != ks) {
#pragma unroll
        for (int h = 0; h < 3; ++h) {
          const u32x4 v = {pk_bf16(acc[2 * h][j][0], acc[2 * h][j][1]), pk_bf16(acc[2 * h][j][2], acc[2 * h][j][3]),
                           pk_bf16(acc[2 * h + 1][j][0], acc[2 * h + 1][j][1]), pk_bf16(acc[2 * h + 1][j][2], acc[2 * h + 1][j][3])};
          __builtin_amdgcn_raw_buffer_store_b128(v, prs, l16 + ((((tile * 4 + j) * 4 + ks) * 4 + w) * 3 + h) * 1024, 0, 16);
        }
      }
    }
    f32x4 own[6];
#pragma unroll
    for (int i = 0; i < 6; ++i) own[i] = ks == 0 ? acc[i][0] : ks == 1 ? acc[i][1] : ks == 2 ? acc[i][2] : acc[i][3];
    const float mb = bf_lo(stw), rsd = 1.f / bf_hi(stw);
    float xf[6][4], xn[6][4];
#pragma unroll
    for (int i = 0; i < 6; ++i)
#pragma unroll
      for (int e = 0; e < 4; ++e) {
        const unsigned xw = xv[i][e >> 1];
        xf[i][e] = (e & 1) ? bf_hi(xw) : bf_lo(xw);
        xn[i][e] = rbf(rbf(xf[i][e] - mb) * rsd);
      }
    WAIT_V(0);
    if (lane == 0) {
      if (__hip_atomic_fetch_add(lcnt, 1u, __ATOMIC_RELAXED, __HIP_MEMORY_SCOPE_WORKGROUP) == 3u) __hip_atomic_fetch_add(pc, 1u, RLX_AGENT);
    }
    STAMP_AT();
    BAR();
    STAMP_AT();
#pragma unroll
    for (int i = 0; i < 6; ++i)
#pragma unroll
      for (int e = 0; e < 4; ++e) {
        const float y2 = bf_lo(*(const u16*)(smem + G_Y + (wd * 96 + 16 * i + 4 * q + e) * 64 + wm * 32 + r16 * 2));
        xf[i][e] = rbf(xf[i][e] + rbf(xn[i][e] * y2));
        PIN(xf[i][e]);
      }
    BAR();
    __builtin_amdgcn_fence(__ATOMIC_ACQUIRE, "wavefront");
    STAMP_AT();
    u32x4 pv[3][3];
#pragma unroll
    for (int sidx = 0; sidx < 3; ++sidx) {
      const int ss = sidx + (sidx >= ks ? 1 : 0);
#pragma unroll
      for (int h = 0; h < 3; ++h)
        pv[sidx][h] = __builtin_amdgcn_raw_buffer_load_b128(prs, l16, ((((tile * 4 + ks) * 4 + ss) * 4 + w) * 3 + h) * 1024, 16);
    }
#pragma unroll
    for (int i = 0; i < 6; ++i) {
      f32x4 sum = own[i];
#pragma unroll
      for (int sidx = 0; sidx < 3; ++sidx) {
        const unsigned lo = pv[sidx][i >> 1][(i & 1) * 2], hi = pv[sidx][i >> 1][(i & 1) * 2 + 1];
        sum += f32x4{bf_lo(lo), bf_hi(lo), bf_lo(hi), bf_hi(hi)};
      }
      *(u32x2*)(out + (size_t)row * D_ + col + 16 * i) =
          u32x2{pk_bf16(xf[i][0] + rbf(sum[0]), xf[i][1] + rbf(sum[1])), pk_bf16(xf[i][2] + rbf(sum[2]), xf[i][3] + rbf(sum[3]))};
    }
    STAMP_AT();
  } else {
    const int cw = xcd * 8 + (slot - 24);
    char* lb = smem + w * CV_W;
    char* ls = smem + 8 * CV_W;
    const int c = cw * 9 + w, c9 = cw * 9 + 8;
    {
      u32x4 gh[10], gu[4], sh0, sh1, su;
      const u32x4* hrow = (const u32x4*)(hk + (size_t)c * S_);
#pragma unroll
      for (int n = 0; n < 5; ++n) {
        int m = n * 64 + lane;
        int m0 = m - 2, m1 = m - 1;
        m0 = m0 < 0 ? 0 : (m0 > 255 ? 255 : m0); m1 = m1 < 0 ? 0 : (m1 > 255 ? 255 : m1);
        gh[2 * n] = __builtin_nontemporal_load(hrow + m0);
        gh[2 * n + 1] = __builtin_nontemporal_load(hrow + m1);
      }
      const u32x4* ur = (const u32x4*)(ws + WS_KVR + (size_t)c * S_ * 2);
#pragma unroll
      for (int n = 0; n < 4; ++n) gu[n] = ur[n * 64 + lane];
      const int ms = 32 * w + lane;
      {
        const u32x4* h9 = (const u32x4*)(hk + (size_t)c9 * S_);
        int m0 = ms - 2, m1 = ms - 1;
        m0 = m0 < 0 ? 0 : (m0 > 255 ? 255 : m0); m1 = m1 < 0 ? 0 : (m1 > 255 ? 255 : m1);
        sh0 = __builtin_nontemporal_load(h9 + m0); sh1 = __builtin_nontemporal_load(h9 + m1);
        su = ((const u32x4*)(ws + WS_KVR + (size_t)c9 * S_ * 2))[ms > 255 ? 255 : ms];
      }
      if (lane < 32) *(u32x4*)(lb + CV_U + 4096 + lane * 16) = u32x4{0u, 0u, 0u, 0u};
      if (w == 0 && lane < 32) *(u32x4*)(ls + CV_U + 4096 + lane * 16) = u32x4{0u, 0u, 0u, 0u};
      auto build = [&](char* base, int m, u32x4 g0, u32x4 g1, bool on) {
        if (m < 2) g0 = u32x4{0u, 0u, 0u, 0u};
        if (m < 1 || m > 256) g1 = u32x4{0u, 0u, 0u, 0u};
        unsigned G[5] = {g0[0], g0[1], g0[2], g0[3], g1[0]};
        unsigned O[4];
#pragma unroll
        for (int d = 0; d < 4; ++d) O[d] = __builtin_amdgcn_alignbit(G[d + 1], G[d], 16);
        if (on) {
          *(u32x4*)(base + m * 16) = u32x4{O[0], O[1], O[2], O[3]};
          *(u32x4*)(base + m * 16 + CV_COPY) = u32x4{G[1], G[2], G[3], G[4]};
        }
      };
#pragma unroll
      for (int n = 0; n < 5; ++n) build(lb, n * 64 + lane, gh[2 * n], gh[2 * n + 1], n < 4 || lane < 2);
#pragma unroll
      for (int n = 0; n < 4; ++n) *(u32x4*)(lb + CV_U + n * 1024 + lane * 16) = gu[n];
      build(ls, ms, sh0, sh1, lane < 32 || (w == 7 && lane < 34));
      if (lane < 32) *(u32x4*)(ls + CV_U + ms * 16) = su;
    }
    const float dv = bf_lo(dvec[c]), dv9 = bf_lo(dvec[c9]);
    STAMP_AT();
    const int qo = CV_U + (2032 - 16 * r16 + 8 * q - 32 * 56) * 2;
    const int po = (r16 & 1) * CV_COPY + ((r16 & ~1) + 8 * q) * 2;
    f32x4 acc[8], acs[8];
#pragma unroll
    for (int i = 0; i < 8; ++i) { acc[i] = f32x4{0.f, 0.f, 0.f, 0.f}; acs[i] = acc[i]; }
    bf16x8 Pf[2][8], Qf[2][8];
    auto ld1 = [&](const char* base, int idx, bf16x8& P, bf16x8& Q) {
      const unsigned* p = (const unsigned*)(base + po + idx * 64);
      P = __builtin_bit_cast(bf16x8, u32x4{p[0], p[1], p[2], p[3]});
      Q = lds_b128(base + qo + idx * 64);
    };
#pragma unroll
    for (int a = 0; a < 8; ++a) ld1(lb, 8 * a, Pf[0][a], Qf[0][a]);
    const char* lsw = ls + w * 64;
    SB();
    static_for<0, 9>([&](auto rc) {
      constexpr int rr = decltype(rc)::value, p = rr & 1;
      if constexpr (rr == 7) { WAIT_L0(); BAR(); }
      static_for<0, 8>([&](auto bc) {
        constexpr int bb = decltype(bc)::value;
        static_for<0, bb + 1>([&](auto ac) {
          constexpr int a = decltype(ac)::value;
          if constexpr (rr < 8) acc[a - bb + 7] = MFMA16(Pf[p][a], Qf[p][bb], acc[a - bb + 7]);
          else acs[a - bb + 7] = MFMA16(Pf[p][a], Qf[p][bb], acs[a - bb + 7]);
        });
        if constexpr (rr < 7) ld1(lb, 8 * bb + rr + 1, Pf[p ^ 1][bb], Qf[p ^ 1][bb]);
        else if constexpr (rr == 7) ld1(lsw, 8 * bb, Pf[p ^ 1][bb], Qf[p ^ 1][bb]);
        SB();
      });
    });
    STAMP_AT();
    auto finish = [&](const char* base, int ch, float d, int i, const f32x4& a) {
      const int tq = 256 * i + 16 * r16 + 4 * q;
      const u32x2 uv = *(const u32x2*)(base + CV_U + (2044 - tq) * 2);
      const float u0 = bf_hi(uv[1]), u1 = bf_lo(uv[1]), u2 = bf_hi(uv[0]), u3 = bf_lo(uv[0]);
      const u32x2 o = {pk_bf16(__builtin_fmaf(u0, d, a[0]), __builtin_fmaf(u1, d, a[1])),
                       pk_bf16(__builtin_fmaf(u2, d, a[2]), __builtin_fmaf(u3, d, a[3]))};
      __builtin_amdgcn_raw_buffer_store_b64(o, yrs, tq * 2, ch * (S_ * 2), 16);
    };
#pragma unroll
    for (int i = 0; i < 8; ++i) finish(lb, c, dv, i, acc[i]);
#pragma unroll
    for (int i = 0; i < 8; ++i) *(f32x4*)(lb + i * 1024 + lane * 16) = acs[i];
    WAIT_L0(); BAR();
    {
      f32x4 a = {0.f, 0.f, 0.f, 0.f};
#pragma unroll
      for (int k = 0; k < 8; ++k) a += *(const f32x4*)(smem + k * CV_W + w * 1024 + lane * 16);
      finish(ls, c9, dv9, w, a);
    }
    WAIT_V(0);
    unsigned arr = 0;
    if (lane == 0) arr = __hip_atomic_fetch_add(lcnt, 1u, __ATOMIC_RELAXED, __HIP_MEMORY_SCOPE_WORKGROUP);
    if (__builtin_amdgcn_readfirstlane(arr) == 7u && lane < 8) __hip_atomic_fetch_add((GAS unsigned*)(ws + WS_CNT + lane * 256), 1u, RLX_AGENT);
    STAMP_AT();
  }
}

extern "C" void kernel_launch(void* const* d_in, const int* in_sizes, int n_in, void* d_out, int out_size, void* d_ws, size_t ws_size,
                              hipStream_t stream) {
  const u16* x = (const u16*)d_in[0];
  const u16* kin = (const u16*)d_in[1];
  const u16* vin = (const u16*)d_in[2];
  const u16* sk = (const u16*)d_in[3];
  const u16* hk = (const u16*)d_in[4];
  const u16* dv = (const u16*)d_in[5];
  const u16* w1 = (const u16*)d_in[7];
  const u16* w3 = (const u16*)d_in[8];
  const u16* w2 = (const u16*)d_in[9];
  k1<<<256, 512, 0, stream>>>(x, w1, w3, w2, kin, vin, sk, (char*)d_ws);
  k2<<<256, 512, 0, stream>>>(x, hk, dv, (char*)d_ws, (u16*)d_out);
}
```

```cpp
#include <hip/hip_runtime.h>
#include <stdint.h>

typedef unsigned short u16;
typedef __attribute__((ext_vector_type(8))) __bf16 bf16x8;
typedef __attribute__((ext_vector_type(2))) __bf16 bf16x2;
typedef __attribute__((ext_vector_type(4))) short s16x4;
typedef __attribute__((ext_vector_type(4))) float f32x4;
typedef __attribute__((ext_vector_type(2))) float f32x2;
typedef __attribute__((ext_vector_type(4))) unsigned u32x4;
typedef __attribute__((ext_vector_type(2))) unsigned u32x2;
#define LAS __attribute__((address_space(3)))
#define GAS __attribute__((address_space(1)))
#define DI __device__ __forceinline__

constexpr int S_ = 2048, D_ = 576, F_ = 4608;
constexpr size_t WS_H = 0;
constexpr size_t WS_Y2 = WS_H + (size_t)128 * 144 * 1024;
constexpr size_t WS_STAT = WS_Y2 + (size_t)D_ * S_ * 2;
constexpr size_t WS_CNT = WS_STAT + (size_t)S_ * 8;
constexpr size_t WS_EPOCH = WS_CNT + 64 * 256;
constexpr size_t WS_DONE = WS_EPOCH + 1024;
constexpr size_t WS_KVF = WS_EPOCH + 4096;
constexpr size_t WS_KVR = WS_KVF + 64 * 256;
constexpr size_t WS_PART = WS_KVR + (size_t)D_ * S_ * 2;

constexpr int ROTK = 1;
constexpr int K1PF = 3;

DI float bf_lo(unsigned v) { return __uint_as_float(v << 16); }
DI float bf_hi(unsigned v) { return __uint_as_float(v & 0xffff0000u); }
DI unsigned pk_bf16(float a, float b) { f32x2 f = {a, b}; bf16x2 r = __builtin_convertvector(f, bf16x2); return __builtin_bit_cast(unsigned, r); }
DI float rbf(float a) { return bf_lo(pk_bf16(a, a)); }
#define WAIT_V(n) asm volatile("s_waitcnt vmcnt(" #n ")" ::: "memory")
#define WAIT_L0() asm volatile("s_waitcnt lgkmcnt(0)" ::: "memory")
#define BAR() do { __builtin_amdgcn_s_barrier(); asm volatile("" ::: "memory"); } while (0)
#define SB() __builtin_amdgcn_sched_barrier(0)
#define PIN(x) asm volatile("" : "+v"(x))
#define MFMA16(a, b, c) __builtin_amdgcn_mfma_f32_16x16x32_bf16((a), (b), (c), 0, 0, 0)

#define MFMA_ACC(acc, a, b) asm volatile("v_mfma_f32_16x16x32_bf16 %0, %1, %2, %0" : "+v"(acc) : "v"(a), "v"(b))
#define MFMA_NEW(acc, a, b) asm volatile("v_mfma_f32_16x16x32_bf16 %0, %1, %2, 0" : "=&v"(acc) : "v"(a), "v"(b))
template <int N> struct IC { static constexpr int value = N; };
template <int I, int N, class Fn> DI void static_for(Fn&& f) {
  if constexpr (I < N) { f(IC<I>{}); static_for<I + 1, N>(f); }
}
template <int CTRL> DI float dpp_add(float v) {
  int r = __builtin_amdgcn_update_dpp(0, __float_as_int(v), CTRL, 0xf, 0xf, false);
  return v + __int_as_float(r);
}
DI float row16_sum(float v) {
  v = dpp_add<0x128>(v); v = dpp_add<0x124>(v); v = dpp_add<0x122>(v); v = dpp_add<0x121>(v);
  return v;
}
template <int NT> DI void glds16a(const void* sbase, int voff, unsigned la) {
  unsigned keep;
  if (NT == 2)
    asm volatile("s_mov_b32 %0, m0\n\ts_mov_b32 m0, %3\n\ts_nop 0\n\tglobal_load_lds_dwordx4 %1, %2 sc1\n\ts_mov_b32 m0, %0"
                 : "=&s"(keep) : "v"(voff), "s"(sbase), "s"(la) : "memory");
  else if (NT)
    asm volatile("s_mov_b32 %0, m0\n\ts_mov_b32 m0, %3\n\ts_nop 0\n\tglobal_load_lds_dwordx4 %1, %2 nt\n\ts_mov_b32 m0, %0"
                 : "=&s"(keep) : "v"(voff), "s"(sbase), "s"(la) : "memory");
  else
    asm volatile("s_mov_b32 %0, m0\n\ts_mov_b32 m0, %3\n\ts_nop 0\n\tglobal_load_lds_dwordx4 %1, %2\n\ts_mov_b32 m0, %0"
                 : "=&s"(keep) : "v"(voff), "s"(sbase), "s"(la) : "memory");
}
DI void glds16a_lo(const void* sbase, int voff, unsigned la) {
  unsigned keep;
  asm volatile("s_mov_b32 %0, m0\n\ts_mov_b32 m0, %3\n\ts_mov_b32 exec_hi, 0\n\tglobal_load_lds_dwordx4 %1, %2\n\ts_mov_b32 exec_hi, -1\n\ts_mov_b32 m0, %0"
               : "=&s"(keep) : "v"(voff), "s"(sbase), "s"(la) : "memory");
}
DI bf16x8 lds_b128(const char* p) { return *(const bf16x8*)p; }
DI s16x4 lds_tr(const char* p) { return __builtin_amdgcn_ds_read_tr16_b64_v4i16((LAS s16x4*)p); }
DI bf16x8 cat_tr(s16x4 lo, s16x4 hi) {
  auto v = __builtin_shufflevector(lo, hi, 0, 1, 2, 3, 4, 5, 6, 7);
  return __builtin_bit_cast(bf16x8, v);
}

constexpr int XTB = 36864;
constexpr int XT = XTB + 512;
constexpr int WB0 = 2 * XT;
constexpr int WCH = 20480;
constexpr int STB = WB0 + 4 * WCH;

struct Acc { f32x4 v[2][2]; };

typedef GAS unsigned long long gu64;
#define RLX_AGENT __ATOMIC_RELAXED, __HIP_MEMORY_SCOPE_AGENT

constexpr int G_STG = 20480, G_NB = 7;
constexpr int G_CNT = G_STG * G_NB;

DI void phase2(char* smem, const u16* __restrict__ x, const u16* __restrict__ w2, char* __restrict__ ws, u16* __restrict__ out, const unsigned tag, const int w) {
  int lane = __builtin_amdgcn_mbcnt_hi(~0u, __builtin_amdgcn_mbcnt_lo(~0u, 0u));
  PIN(lane);
  const int tid = w * 64 + lane;
  const int b = blockIdx.x, xcd = b & 7, slot = b >> 3;
  const int r16 = lane & 15, q = lane >> 4;
  LAS unsigned* lcnt = (LAS unsigned*)(smem + G_CNT);
  const int ks = xcd & 3, Mt = (xcd >> 2) * 8 + slot / 3, Dt = slot % 3;
  const int tile = Mt * 3 + Dt;
  const int l16 = lane * 16;
  GAS unsigned* pf = (GAS unsigned*)(ws + WS_CNT + tile * 256);
  if (tid == 0) *lcnt = 0u;
  if (w >= 4) {
    const int v = w - 4;
    const unsigned lds0 = (unsigned)(uintptr_t)(LAS char*)smem + v * 1024;
    const char* sh = ws + WS_H + ((size_t)((8 * Mt + v) * 144 + 36 * ks)) * 1024;
    const char* sw = (const char*)(w2 + (size_t)(36 * ks * 32) * D_ + Dt * 192);
    int wo[3];
#pragma unroll
    for (int n = 0; n < 3; ++n) {
      const int p = (v + 4 * n) * 64 + lane, row = p / 24, pos = p % 24;
      const int f = ((row >> 1) & 1) | (((row >> 3) & 1) << 1);
      wo[n] = row * (D_ * 2) + (pos ^ (f << 1)) * 16;
    }
    auto issue_h = [&](int st, int buf) {
      const unsigned l = lds0 + buf * G_STG;
      glds16a<2>(sh + st * 1024, l16, l);
      glds16a<2>(sh + st * 1024 + 4 * 144 * 1024, l16, l + 4096);
    };
    auto issue_w = [&](int st, int buf) {
      const unsigned l = lds0 + buf * G_STG;
      glds16a<0>(sw + st * (32 * D_ * 2), wo[0], l + 8192);
      glds16a<0>(sw + st * (32 * D_ * 2), wo[1], l + 12288);
      glds16a<0>(sw + st * (32 * D_ * 2), wo[2], l + 16384);
    };
    auto issue = [&](int st, int buf) { issue_h(st, buf); issue_w(st, buf); };
#pragma unroll
    for (int st = 0; st < 6; ++st) issue_w(st, st);
    {
      GAS unsigned* df = (GAS unsigned*)(ws + WS_DONE) + 54 * ks + (lane < 54 ? lane : 0);
      for (unsigned spins = 0; spins < (1u << 20); ++spins) {
        if (__all(__hip_atomic_load(df, RLX_AGENT) == tag)) break;
        __builtin_amdgcn_s_sleep(3);
      }
      __builtin_amdgcn_fence(__ATOMIC_ACQUIRE, "wavefront");
    }
#pragma unroll
    for (int st = 0; st < 6; ++st) issue_h(st, st);
    WAIT_V(10);
    BAR();
    int be = 0;
#pragma unroll 1
    for (int e = 0; e < 36; e += 2) {
      if (e == 0) WAIT_V(6); else if (e == 2) WAIT_V(12); else if (e <= 30) WAIT_V(15); else if (e == 32) WAIT_V(5); else WAIT_V(0);
      BAR();
      if (e + 6 < 36) { issue(e + 6, be == 0 ? G_NB - 1 : be - 1); issue(e + 7, be); }
      be = be + 2 >= G_NB ? be + 2 - G_NB : be + 2;
    }
    if (w == 4) {
      GAS unsigned* cf = (GAS unsigned*)(ws + WS_DONE) + 256 + 12 * Dt + (lane < 12 ? lane : 0);
      for (unsigned spins = 0; spins < (1u << 20); ++spins) {
        if (__all(__hip_atomic_load(cf, RLX_AGENT) == tag)) break;
        __builtin_amdgcn_s_sleep(4);
      }
    }
    BAR();
    if (w == 5) {
      __builtin_amdgcn_s_sleep(16);
      for (unsigned spins = 0; spins < (1u << 22); ++spins) {
        if (__all(__hip_atomic_load(pf + (lane & 3), RLX_AGENT) == tag)) break;
        __builtin_amdgcn_s_sleep(1);
      }
    }
    BAR();
    return;
  }
  const int wm = w >> 1, wd = w & 1;
  const int row = Mt * 128 + wm * 64 + ks * 16 + r16, col = Dt * 192 + wd * 96 + 4 * q;
  f32x4 acc[6][4];
#pragma unroll
  for (int i = 0; i < 6; ++i)
#pragma unroll
    for (int j = 0; j < 4; ++j) acc[i][j] = f32x4{0.f, 0.f, 0.f, 0.f};
  const int rb = 4 * wm * 1024 + l16;
  int ra[4];
  {
    const int rr = r16 >> 2, p4 = lane & 3, krow = 8 * q + rr;
    const int f = ((krow >> 1) & 1) | (((krow >> 3) & 1) << 1);
#pragma unroll
    for (int v = 0; v < 4; ++v) ra[v] = 8192 + krow * 384 + ((v ^ f) * 32) + p4 * 8;
  }
  int rs_[6];
#pragma unroll
  for (int i = 0; i < 6; ++i) rs_[i] = wd ? ra[(6 + i) & 3] + ((6 + i) >> 2) * 128 : ra[i & 3] + (i >> 2) * 128;
  bf16x8 fa[2][6], fb[2][4];
  auto rd_a = [&](const char* base, int i) { return cat_tr(lds_tr(base + rs_[i]), lds_tr(base + rs_[i] + 4 * 384)); };
  BAR();
#pragma unroll
  for (int j = 0; j < 4; ++j) fb[0][j] = lds_b128(smem + rb + j * 1024);
#pragma unroll
  for (int i = 0; i < 6; ++i) fa[0][i] = rd_a(smem, i);
  int be = 0;
#pragma unroll 1
  for (int e = 0; e < 36; e += 2) {
    WAIT_L0();
    BAR();
    const int b1 = be + 1 >= G_NB ? be + 1 - G_NB : be + 1, b2 = be + 2 >= G_NB ? be + 2 - G_NB : be + 2;
    SB();
    static_for<0, 2>([&](auto pcn) {
      constexpr int p = decltype(pcn)::value;
      const char* ln = smem + (p ? b2 : b1) * G_STG;
      s16x4 tl[6], th[6];
      static_for<0, 24>([&](auto kc) {
        constexpr int k = decltype(kc)::value, i = k >> 2, j = k & 3;
        MFMA_ACC(acc[i][j], fa[p][i], fb[p][j]);
        if constexpr (k < 4) fb[p ^ 1][k] = lds_b128(ln + rb + k * 1024);
        else if constexpr (k < 16 && !(k & 1)) tl[(k - 4) >> 1] = lds_tr(ln + rs_[(k - 4) >> 1]);
        else if constexpr (k < 16) { th[(k - 4) >> 1] = lds_tr(ln + rs_[(k - 4) >> 1] + 4 * 384); fa[p ^ 1][(k - 4) >> 1] = cat_tr(tl[(k - 4) >> 1], th[(k - 4) >> 1]); }
        SB();
      });
    });
    be = b2;
  }
  asm volatile("s_nop 15" ::: "memory");
  SB();
  const __amdgpu_buffer_rsrc_t prs = __builtin_amdgcn_make_buffer_rsrc(ws + WS_PART, 0, 48 * 16 * 12 * 1024, 0x00020000);
#pragma unroll
  for (int j = 0; j < 4; ++j) {
    if (j != ks) {
#pragma unroll
      for (int h = 0; h < 3; ++h) {
        const u32x4 v = {pk_bf16(acc[2 * h][j][0], acc[2 * h][j][1]), pk_bf16(acc[2 * h][j][2], acc[2 * h][j][3]),
                         pk_bf16(acc[2 * h + 1][j][0], acc[2 * h + 1][j][1]), pk_bf16(acc[2 * h + 1][j][2], acc[2 * h + 1][j][3])};
        __builtin_amdgcn_raw_buffer_store_b128(v, prs, l16 + ((((tile * 4 + j) * 4 + ks) * 4 + w) * 3 + h) * 1024, 0, 16);
      }
    }
  }
  f32x4 own[6];
#pragma unroll
  for (int i = 0; i < 6; ++i) own[i] = ks == 0 ? acc[i][0] : ks == 1 ? acc[i][1] : ks == 2 ? acc[i][2] : acc[i][3];
  BAR();
  __builtin_amdgcn_fence(__ATOMIC_ACQUIRE, "wavefront");
  SB();
  u32x2 xv[6], yv[6];
  const __amdgpu_buffer_rsrc_t yrs = __builtin_amdgcn_make_buffer_rsrc(ws + WS_Y2, 0, D_ * S_ * 2 + S_ * 8, 0x00020000);
#pragma unroll
  for (int i = 0; i < 6; ++i) {
    xv[i] = *((const u32x2*)(x + (size_t)row * D_ + col + 16 * i));
    yv[i] = __builtin_amdgcn_raw_buffer_load_b64(yrs, ((Dt * 12 + wd * 6 + i) * S_ + row) * 32 + q * 8, 0, 16);
  }
  const unsigned stw = __builtin_amdgcn_raw_buffer_load_b32(yrs, D_ * S_ * 2 + row * 8, 0, 16);
  SB();
  WAIT_V(13);
  if (lane == 0) {
    if (__hip_atomic_fetch_add(lcnt, 1u, __ATOMIC_RELAXED, __HIP_MEMORY_SCOPE_WORKGROUP) == 3u) __hip_atomic_store(pf + ks, tag, RLX_AGENT);
  }
  u32x2 rr_[6];
  {
    const float mb = bf_lo(stw), rsd = 1.f / bf_hi(stw);
#pragma unroll
    for (int i = 0; i < 6; ++i)
#pragma unroll
      for (int h = 0; h < 2; ++h) {
        const unsigned xw = xv[i][h], yw = yv[i][h];
        const unsigned d = pk_bf16(bf_lo(xw) - mb, bf_hi(xw) - mb);
        const unsigned xn = pk_bf16(bf_lo(d) * rsd, bf_hi(d) * rsd);
        const unsigned a = pk_bf16(bf_lo(xn) * bf_lo(yw), bf_hi(xn) * bf_hi(yw));
        rr_[i][h] = pk_bf16(bf_lo(xw) + bf_lo(a), bf_hi(xw) + bf_hi(a));
        PIN(rr_[i][h]);
      }
  }
  BAR();
  __builtin_amdgcn_fence(__ATOMIC_ACQUIRE, "wavefront");
  u32x4 pv[3][3];
#pragma unroll
  for (int sidx = 0; sidx < 3; ++sidx) {
    const int ss = sidx + (sidx >= ks ? 1 : 0);
#pragma unroll
    for (int h = 0; h < 3; ++h)
      pv[sidx][h] = __builtin_amdgcn_raw_buffer_load_b128(prs, l16, ((((tile * 4 + ks) * 4 + ss) * 4 + w) * 3 + h) * 1024, 16);
  }
#pragma unroll
  for (int i = 0; i < 6; ++i) {
    f32x4 sum = own[i];
#pragma unroll
    for (int sidx = 0; sidx < 3; ++sidx) {
      const unsigned lo = pv[sidx][i >> 1][(i & 1) * 2], hi = pv[sidx][i >> 1][(i & 1) * 2 + 1];
      sum += f32x4{bf_lo(lo), bf_hi(lo), bf_lo(hi), bf_hi(hi)};
    }
    const unsigned b0 = pk_bf16(sum[0], sum[1]), b1 = pk_bf16(sum[2], sum[3]);
    *(u32x2*)(out + (size_t)row * D_ + col + 16 * i) =
        u32x2{pk_bf16(bf_lo(rr_[i][0]) + bf_lo(b0), bf_hi(rr_[i][0]) + bf_hi(b0)), pk_bf16(bf_lo(rr_[i][1]) + bf_lo(b1), bf_hi(rr_[i][1]) + bf_hi(b1))};
  }
  asm volatile(".Lk1_code_hi:");
}


constexpr int CV_UB = 4608, CV_COPY = 4160, CV_H = 16 * CV_UB;

__global__ __launch_bounds__(512, 2) void k1(const u16* __restrict__ x, const u16* __restrict__ w1, const u16* __restrict__ w3,
                                             const u16* __restrict__ kin, const u16* __restrict__ vin, const u16* __restrict__ sk,
                                             const u16* __restrict__ hk, const u16* __restrict__ dvec, const u16* __restrict__ w2,
                                             char* __restrict__ ws, u16* __restrict__ out) {
  __shared__ __attribute__((aligned(16))) char smem[STB + 5632];
  const int tid = threadIdx.x, lane = tid & 63;
  const int w = __builtin_amdgcn_readfirstlane(tid >> 6);
  const int b = blockIdx.x, xcd = b & 7, slot = b >> 3;
  const bool gemm = slot < 27;
  const int L = xcd * 27 + slot;
  const int fg = L / 6, mg = L % 6;
  const int t0 = mg < 5 ? 11 * mg : 54, nt = mg < 4 ? 11 : 10;
  const int r16 = lane & 15, q = lane >> 4;
  const int rot = mg * ROTK;
  unsigned long long pc; unsigned ext;
  asm volatile("s_getpc_b64 %0\n.Lk1_code_lo:\n\ts_mov_b32 %1, .Lk1_code_hi-.Lk1_code_lo" : "=s"(pc), "=s"(ext));
  auto code_prefetch = [&]() {
    unsigned keep;
    int off = tid * 128; off = off < (int)ext - 4 ? off : (int)ext - 4;
    asm volatile("s_mov_b32 %0, m0\n\ts_mov_b32 m0, %3\n\ts_nop 0\n\tglobal_load_lds_dword %1, %2\n\ts_mov_b32 m0, %0"
                 : "=&s"(keep) : "v"(off), "s"(pc), "s"((unsigned)(uintptr_t)(LAS char*)smem + STB + w * 256) : "memory");
  };

  gu64* const gran = (gu64*)(ws + WS_STAT);
  unsigned tag = *(const __attribute__((address_space(4))) unsigned*)(ws + WS_EPOCH) + 1u;
  auto all_row_stats = [&]() {
    const int gw = (39 - (xcd * 5 + slot - 27)) * 8 + w;
    if (gw >= 256) return;
    u32x2 xr[2][9];
#pragma unroll
    for (int k = 0; k < 2; ++k) {
      const u32x2* xrow = (const u32x2*)(x + (size_t)(gw * 8 + k * 4 + q) * D_) + r16;
#pragma unroll
      for (int i = 0; i < 9; ++i) xr[k][i] = xrow[i * 16];
    }
#pragma unroll
    for (int k = 0; k < 2; ++k) {
      float s0 = 0.f, s1 = 0.f;
#pragma unroll
      for (int i = 0; i < 9; ++i) { s0 += bf_lo(xr[k][i][0]) + bf_hi(xr[k][i][0]); s1 += bf_lo(xr[k][i][1]) + bf_hi(xr[k][i][1]); }
      const float mean = row16_sum(s0 + s1) * (1.f / 576.f);
      float q0 = 0.f, q1 = 0.f;
#pragma unroll
      for (int i = 0; i < 9; ++i) {
        const float d0 = bf_lo(xr[k][i][0]) - mean, d1 = bf_hi(xr[k][i][0]) - mean, d2 = bf_lo(xr[k][i][1]) - mean, d3 = bf_hi(xr[k][i][1]) - mean;
        q0 += d0 * d0 + d1 * d1; q1 += d2 * d2 + d3 * d3;
      }
      const float sd = sqrtf(rbf(rbf(row16_sum(q0 + q1) * (1.f / 576.f)) + 1e-5f));
      if (r16 == 0) __hip_atomic_store(gran + gw * 8 + k * 4 + q, ((unsigned long long)tag << 32) | pk_bf16(mean, sd), RLX_AGENT);
    }
  };
  auto advance_epoch = [&]() {
    GAS unsigned* df = (GAS unsigned*)(ws + WS_DONE);
    for (unsigned spins = 0; spins < (1u << 20); ++spins) {
      bool ok = true;
#pragma unroll
      for (int i = 0; i < 4; ++i) ok &= __hip_atomic_load(df + (i * 64 + lane < 216 ? i * 64 + lane : 0), RLX_AGENT) == tag;
      ok &= __hip_atomic_load(df + 256 + (lane < 40 ? lane : 0), RLX_AGENT) == tag;
      if (__all(ok)) break;
      __builtin_amdgcn_s_sleep(64);
    }
    if (lane == 0) __hip_atomic_store((GAS unsigned*)(ws + WS_EPOCH), tag, RLX_AGENT);
  };
  if (!gemm) {
    code_prefetch();
    asm volatile("" : "+s"(tag));
    all_row_stats();
    if (slot == 31 && xcd >= 4) {
      BAR();
      if (tid == 0) __hip_atomic_store((GAS unsigned*)(ws + WS_DONE) + 256 + 32 + xcd, tag, RLX_AGENT);
      if (xcd == 7 && w == 0) advance_epoch();
      return;
    }
    const int cg = slot < 31 ? xcd * 4 + (slot - 27) : 32 + xcd;
    const int c0 = cg * 16;
    const int half = tid & 1, tr0 = (tid >> 1) * 8;
    u32x4 gh[10];
    auto fetch_h = [&](int c) {
      const u32x4* hrow = (const u32x4*)(hk + (size_t)c * S_);
#pragma unroll
      for (int n = 0; n < 5; ++n) {
        int m = n * 64 + lane;
        int m0 = m - 2, m1 = m - 1;
        m0 = m0 < 0 ? 0 : (m0 > 255 ? 255 : m0); m1 = m1 < 0 ? 0 : (m1 > 255 ? 255 : m1);
        gh[2 * n] = *(hrow + m0);
        gh[2 * n + 1] = *(hrow + m1);
      }
    };
    char* lh = smem + CV_H + w * (2 * CV_COPY);
    auto build_h = [&]() {
#pragma unroll
      for (int n = 0; n < 5; ++n) {
        const int m = n * 64 + lane;
        u32x4 g0 = gh[2 * n], g1 = gh[2 * n + 1];
        if (n == 0) { if (m < 2) g0 = u32x4{0u, 0u, 0u, 0u}; if (m < 1) g1 = u32x4{0u, 0u, 0u, 0u}; }
        if (n == 4) { if (m > 256) g1 = u32x4{0u, 0u, 0u, 0u}; }
        unsigned G[5] = {g0[0], g0[1], g0[2], g0[3], g1[0]};
        unsigned O[4];
#pragma unroll
        for (int d = 0; d < 4; ++d) O[d] = __builtin_amdgcn_alignbit(G[d + 1], G[d], 16);
        if (n < 4 || lane < 2) {
          *(u32x4*)(lh + m * 16) = u32x4{O[0], O[1], O[2], O[3]};
          *(u32x4*)(lh + m * 16 + CV_COPY) = u32x4{G[1], G[2], G[3], G[4]};
        }
      }
    };
    {
      u32x4 kr[11], vr[8], skv[4];
      const int cc = (cg >> 2) * 64 + (tid & 7) * 8, tk = (cg & 3) * 512 + (tid >> 3) * 8;
#pragma unroll
      for (int r = 0; r < 11; ++r) { int t = tk - 3 + r; kr[r] = *((const u32x4*)(kin + (size_t)(t < 0 ? 0 : t) * D_ + cc)); }
#pragma unroll
      for (int r = 0; r < 8; ++r) vr[r] = *((const u32x4*)(vin + (size_t)(tk + r) * D_ + cc));
#pragma unroll
      for (int p = 0; p < 4; ++p) skv[p] = *(const u32x4*)(sk + (cc + 2 * p) * 4);
      fetch_h(c0 + w);
      *(u32x4*)(smem + (tid >> 5) * CV_UB + 4096 + (tid & 31) * 16) = u32x4{0u, 0u, 0u, 0u};
      if (tk == 0) { kr[0] = u32x4{0u, 0u, 0u, 0u}; kr[1] = kr[0]; kr[2] = kr[0]; }
      const __amdgpu_buffer_rsrc_t krs = __builtin_amdgcn_make_buffer_rsrc(ws + WS_KVR, 0, D_ * S_ * 2, 0x00020000);
#pragma unroll
      for (int p = 0; p < 4; ++p) {
        const float sa[4] = {bf_hi(skv[p][1]), bf_lo(skv[p][1]), bf_hi(skv[p][0]), bf_lo(skv[p][0])};
        const float sb[4] = {bf_hi(skv[p][3]), bf_lo(skv[p][3]), bf_hi(skv[p][2]), bf_lo(skv[p][2])};
        unsigned res[8];
        float kl[11], kh[11];
#pragma unroll
        for (int r = 0; r < 11; ++r) { kl[r] = bf_lo(kr[r][p]); kh[r] = bf_hi(kr[r][p]); }
#pragma unroll
        for (int r = 0; r < 8; ++r) {
          float al = kl[r] * sa[0], ah = kh[r] * sb[0];
#pragma unroll
          for (int j = 1; j < 4; ++j) { al = __builtin_fmaf(kl[r + j], sa[j], al); ah = __builtin_fmaf(kh[r + j], sb[j], ah); }
          res[r] = pk_bf16(al * bf_lo(vr[r][p]), ah * bf_hi(vr[r][p]));
        }
        u32x4 oa, ob;
#pragma unroll
        for (int d = 0; d < 4; ++d) {
          const unsigned e0 = res[7 - 2 * d], e1 = res[6 - 2 * d];
          oa[d] = (e0 & 0xffffu) | (e1 << 16);
          ob[d] = (e0 >> 16) | (e1 & 0xffff0000u);
        }
        const int off = ((cc + 2 * p) * S_ + 2040 - tk) * 2;
        __builtin_amdgcn_raw_buffer_store_b128(oa, krs, off, 0, 16);
        __builtin_amdgcn_raw_buffer_store_b128(ob, krs, off + S_ * 2, 0, 16);
      }
      build_h();
      WAIT_V(0);
      WAIT_L0();
      BAR();
      GAS unsigned* kf = (GAS unsigned*)(ws + WS_KVF);
      if (tid == 0) __hip_atomic_store(kf + cg * 64, tag, RLX_AGENT);
      if (w == 0) {
        for (unsigned spins = 0; spins < (1u << 20); ++spins) {
          const unsigned v = __hip_atomic_load(kf + ((cg & ~3) + (lane & 3)) * 64, RLX_AGENT);
          if (__all(v == tag)) break;
          __builtin_amdgcn_s_sleep(2);
        }
      }
      BAR();
      __builtin_amdgcn_fence(__ATOMIC_ACQUIRE, "wavefront");
      u32x4 uu[8];
#pragma unroll
      for (int n = 0; n < 8; ++n)
        uu[n] = __builtin_amdgcn_raw_buffer_load_b128(krs, (n & 3) * 1024 + lane * 16, (c0 + (n >> 2) * 8 + w) * (S_ * 2), 16);
#pragma unroll
      for (int n = 0; n < 8; ++n) *(u32x4*)(smem + ((n >> 2) * 8 + w) * CV_UB + (n & 3) * 1024 + lane * 16) = uu[n];
    }
    const char* pp = lh + (r16 & 1) * CV_COPY + ((r16 & ~1) + 8 * q) * 2;
#pragma unroll 1
    for (int rd = 0; rd < 2; ++rd) {
      char* lu = smem + (8 * rd + w) * CV_UB;
      const char* qp = lu + (2032 - 16 * r16 + 8 * q - 32 * 56) * 2;
      const float dv = bf_lo(dvec[c0 + 8 * rd + w]);
      if (rd == 0) fetch_h(c0 + 8 + w);
      f32x4 acc[8];
#pragma unroll
      for (int i = 0; i < 8; ++i) acc[i] = f32x4{0.f, 0.f, 0.f, 0.f};
      bf16x8 Pf[2][8], Qf[2][8];
      auto ld1 = [&](int idx, bf16x8& P, bf16x8& Q) {
        const unsigned* p = (const unsigned*)(pp + idx * 64);
        P = __builtin_bit_cast(bf16x8, u32x4{p[0], p[1], p[2], p[3]});
        Q = lds_b128(qp + idx * 64);
      };
#pragma unroll
      for (int a = 0; a < 8; ++a) ld1(8 * a, Pf[0][a], Qf[0][a]);
      SB();
      static_for<0, 8>([&](auto rc) {
        constexpr int rr = decltype(rc)::value, p = rr & 1;
        static_for<0, 8>([&](auto bc) {
          constexpr int bb = decltype(bc)::value;
          static_for<0, bb + 1>([&](auto ac) {
            constexpr int a = decltype(ac)::value;
            acc[a - bb + 7] = MFMA16(Pf[p][a], Qf[p][bb], acc[a - bb + 7]);
          });
          if constexpr (rr < 7) ld1(8 * bb + rr + 1, Pf[p ^ 1][bb], Qf[p ^ 1][bb]);
          SB();
        });
      });
#pragma unroll
      for (int i = 0; i < 8; ++i) {
        char* up = lu + (2044 - (256 * i + 16 * r16 + 4 * q)) * 2;
        const u32x2 uv = *(const u32x2*)up;
        *(u32x2*)up = u32x2{pk_bf16(__builtin_fmaf(bf_lo(uv[0]), dv, acc[i][3]), __builtin_fmaf(bf_hi(uv[0]), dv, acc[i][2])),
                            pk_bf16(__builtin_fmaf(bf_lo(uv[1]), dv, acc[i][1]), __builtin_fmaf(bf_hi(uv[1]), dv, acc[i][0]))};
      }
      if (rd == 0) build_h();
    }
    WAIT_L0(); BAR();
    {
      u32x4 in[8];
#pragma unroll
      for (int ch = 0; ch < 8; ++ch) in[ch] = *(const u32x4*)(smem + (8 * half + ch) * CV_UB + (2040 - tr0) * 2);
      char* yl = smem + CV_H + tr0 * 32 + half * 16;
#pragma unroll
      for (int d = 0; d < 4; ++d) {
        u32x4 lo, hi;
#pragma unroll
        for (int p = 0; p < 4; ++p) {
          lo[p] = (in[2 * p][d] & 0xffffu) | (in[2 * p + 1][d] << 16);
          hi[p] = (in[2 * p][d] >> 16) | (in[2 * p + 1][d] & 0xffff0000u);
        }
        *(u32x4*)(yl + (7 - 2 * d) * 32) = lo;
        *(u32x4*)(yl + (6 - 2 * d) * 32) = hi;
      }
      const __amdgpu_buffer_rsrc_t yrs = __builtin_amdgcn_make_buffer_rsrc(ws + WS_Y2, 0, D_ * S_ * 2, 0x00020000);
#pragma unroll
      for (int n = 0; n < 8; ++n) {
        const int o = w * 8192 + n * 1024 + lane * 16;
        __builtin_amdgcn_raw_buffer_store_b128(*(const u32x4*)(smem + CV_H + o), yrs, cg * 65536 + o, 0, 16);
      }
    }
    WAIT_V(0);
    BAR();
    if (tid == 0) __hip_atomic_store((GAS unsigned*)(ws + WS_DONE) + 256 + cg, tag, RLX_AGENT);
    return;
  }
  int wsrc;
  {
    const int k = 4 * w + q, pos = lane & 15;
    const int ls = (pos >> 1) ^ ((k & 3) | (((k >> 3) & 1) << 2));
    wsrc = k * (F_ * 2) + fg * 256 + (ls * 2 + (pos & 1)) * 16;
  }
  int xsrc[5];
  auto make_xsrc = [&]() {
    int lane = __builtin_amdgcn_mbcnt_hi(~0u, __builtin_amdgcn_mbcnt_lo(~0u, 0u)); PIN(lane);
#pragma unroll
    for (int n = 0; n < 5; ++n) {
      const int r = n == 0 ? 0 : n - 1 + (lane >= 8 * n ? 1 : 0);
      const int pos = n * 64 + lane - 72 * r;
      int row = 4 * w + r; if (row > 31) row = 31;
      int c = pos ^ ((row >> 1) & 7);
      c += 4 * rot; if (c >= 72) c -= 72;
      xsrc[n] = row * 1152 + c * 16;
    }
  };
  auto issue_x = [&](int tile, int slot_off) {
    const char* g = (const char*)x + (size_t)tile * XTB;
    const unsigned l = (unsigned)(uintptr_t)(LAS char*)smem + slot_off + w * 4608;
#pragma unroll
    for (int n = 0; n < 5; ++n) glds16a<0>(g, xsrc[n], l + n * 1024);
  };
  const unsigned lds0 = (unsigned)(uintptr_t)(LAS char*)smem;
  const int xcs = (8 * w + (lane >> 2)) * 1152 + (((lane & 3) ^ ((w & 1) << 1)) << 4);
  const char* xcb = (const char*)x + (size_t)t0 * XTB;
  auto issue_sp = [&](int c, int n) {
    int ca = c + rot; if (ca >= 18) ca -= 18;
    if (n < 2) glds16a<0>((const char*)(n ? w3 : w1) + (size_t)ca * (32 * F_ * 2), wsrc, lds0 + WB0 + (c & 3) * WCH + w * 2048 + n * 1024);
    else glds16a_lo(xcb + ca * 64, xcs, lds0 + WB0 + (c & 3) * WCH + 16384 + w * 512);
  };
#pragma unroll
  for (int c = 0; c < 3; ++c) { issue_sp(c, 0); issue_sp(c, 1); issue_sp(c, 2); }
  asm volatile("" : "+s"(tag));

  int xa[4];
  {
    int g = (r16 >> 1) & 7;
#pragma unroll
    for (int v = 0; v < 4; ++v) xa[v] = (r16 + 16 * (v >> 1)) * 1152 + (((4 * (v & 1) + q) ^ g) << 4);
  }
#define XADDR(n) (xa[((n) & 1) * 2 + (((n) >> 1) & 1)] + ((n) >> 2) * 128)
  int wtr;
  {
    int rr = (lane & 15) >> 2, p4 = lane & 3;
    wtr = WB0 + q * 4096 + rr * 256 + ((w ^ (rr | ((q & 1) << 2))) * 32) + p4 * 8;
  }

  bf16x8 W[18][2];
  Acc accA, accB, accC;
  f32x4 cs[2];

  unsigned long long gv;
  auto gather_load = [&]() {
    const int t2 = w * 64 + __builtin_amdgcn_mbcnt_hi(~0u, __builtin_amdgcn_mbcnt_lo(~0u, 0u));
    const int off = (t0 * 32 + (t2 < nt * 32 ? t2 : 0)) * 8;
    asm volatile("global_load_dwordx2 %0, %1, %2 sc1" : "=v"(gv) : "v"(off), "s"(gran) : "memory");
  };
  auto gather_stats = [&]() {
    asm volatile("" : "+v"(gv) :: "memory");
    const int t2 = w * 64 + __builtin_amdgcn_mbcnt_hi(~0u, __builtin_amdgcn_mbcnt_lo(~0u, 0u));
    for (unsigned spins = 0; spins < (1u << 20); ++spins) {
      if (__all((unsigned)(gv >> 32) == tag)) break;
      __builtin_amdgcn_s_sleep(4);
      gv = __hip_atomic_load(gran + t0 * 32 + (t2 < nt * 32 ? t2 : 0), RLX_AGENT);
    }
    const float mb = bf_lo((unsigned)gv), sd = bf_hi((unsigned)gv);
    f32x4 o = {-mb, -1.4426950408889634f * __builtin_amdgcn_rcpf(sd), sd * sd, 0.f};
    if (t2 < 352) *(f32x4*)(smem + STB + t2 * 16) = o;
  };
  constexpr int LAG = 8;
  int xcr = WB0 + 16384 + r16 * 64 + ((q ^ ((r16 >> 3) << 1)) << 4);
  bf16x8 xp_[6];
  static_for<0, 19>([&](auto cc) {
    constexpr int c = decltype(cc)::value;
    constexpr int sa = c - 1, sb = c - 1 - LAG;
    if constexpr (c < 18) {
      if constexpr ((c >= 3 && c <= 5) || (c >= 11 && c <= 13)) WAIT_V(11);
      else if constexpr (c <= 14) WAIT_V(6);
      else if constexpr (c == 15) WAIT_V(7); else if constexpr (c == 16) WAIT_V(4); else WAIT_V(0);
      asm volatile("s_waitcnt lgkmcnt(1)" ::: "memory");
      BAR();
    }
    SB();
    if constexpr (c > 0 && c < 18) {
      wtr += (c & 3) ? WCH : -3 * WCH; xcr += (c & 3) ? WCH : -3 * WCH;
      PIN(wtr); PIN(xcr);
    }
    const char* bp = smem + wtr;
    const char* xq_ = smem + xcr;
    s16x4 a0, a1, b0, b1;
#define MF(cond, first, acc, wa, xb) if constexpr (cond) { if constexpr (first) MFMA_NEW(acc, wa, xb); else MFMA_ACC(acc, wa, xb); }
    constexpr int sa_ = sa < 0 ? 0 : sa, sb_ = sb < 0 ? 0 : sb;
    MF(sb >= 0, sb == 0, accB.v[0][0], W[sb_][0], xp_[4]);
    if constexpr (c < 18) a0 = lds_tr(bp);
    SB();
    MF(sb >= 0, sb == 0, accB.v[0][1], W[sb_][1], xp_[4]);
    if constexpr (sb >= 0) xp_[4] = lds_b128(smem + XADDR(2 * sb_ + 1));
    SB();
    MF(sa >= 0, sa == 0, accC.v[0][0], W[sa_][0], xp_[0]);
    if constexpr (c < 18) a1 = lds_tr(bp + 2048);
    SB();
    MF(sa >= 0, sa == 0, accC.v[0][1], W[sa_][1], xp_[0]);
    if constexpr (c < 18) xp_[0] = lds_b128(xq_);
    SB();
    MF(sa >= 0, sa == 0, accC.v[1][0], W[sa_][0], xp_[1]);
    if constexpr (c < 18) b0 = lds_tr(bp + 1024);
    SB();
    MF(sa >= 0, sa == 0, accC.v[1][1], W[sa_][1], xp_[1]);
    if constexpr (c < 18) xp_[1] = lds_b128(xq_ + 1024);
    SB();
    MF(sa >= 0, sa == 0, accA.v[0][0], W[sa_][0], xp_[2]);
    if constexpr (c < 18) b1 = lds_tr(bp + 2048 + 1024);
    SB();
    MF(sa >= 0, sa == 0, accA.v[0][1], W[sa_][1], xp_[2]);
    if constexpr (c < 18) xp_[2] = lds_b128(xq_ + 2048);
    SB();
    MF(sa >= 0, sa == 0, accA.v[1][0], W[sa_][0], xp_[3]);
    if constexpr (c + 3 < 18) issue_sp(c + 3, 0);
    SB();
    MF(sa >= 0, sa == 0, accA.v[1][1], W[sa_][1], xp_[3]);
    if constexpr (c < 18) xp_[3] = lds_b128(xq_ + 3072);
    SB();
    MF(sb >= 0, sb == 0, accB.v[1][0], W[sb_][0], xp_[4]);
    if constexpr (c + 3 < 18) issue_sp(c + 3, 1);
    SB();
    MF(sb >= 0, sb == 0, accB.v[1][1], W[sb_][1], xp_[4]);
    if constexpr (sb + 1 >= 0) xp_[4] = lds_b128(smem + XADDR(2 * (sb + 1)));
    SB();
    if constexpr (sa >= 0) {
      u32x4 o1 = {0x3f803f80u, 0x3f803f80u, 0x3f803f80u, 0x3f803f80u};
      asm volatile("s_nop 1" : "+v"(o1));
      const bf16x8 ones = __builtin_bit_cast(bf16x8, o1);
      MF(true, sa == 0, cs[0], W[sa_][0], ones);
      if constexpr (c + 3 < 18) issue_sp(c + 3, 2);
      SB();
      MF(true, sa == 0, cs[1], W[sa_][1], ones);
    } else {
      issue_sp(c + 3, 2);
    }
    if constexpr (c == 18) xp_[5] = lds_b128(smem + XADDR(2 * (sb + 1) + 1));
    if constexpr (c == 2) { make_xsrc(); issue_x(t0 + 2, 0); }
    if constexpr (c == 10) { make_xsrc(); issue_x(t0 + 3, XT); }
    if constexpr (c < 18) { W[c][0] = cat_tr(a0, a1); W[c][1] = cat_tr(b0, b1); }
    SB();
    if constexpr (sb + 1 < 0) WAIT_L0();
    if constexpr (c == 14) gather_load();
    if constexpr (c == 18) gather_stats();
  });
  WAIT_L0();
  BAR();
  make_xsrc();
  issue_x(t0 + 4, 2 * XT);

  const __amdgpu_buffer_rsrc_t hrs = __builtin_amdgcn_make_buffer_rsrc(ws + WS_H, 0, 128 * 144 * 1024, 0x00020000);
  int ln2 = __builtin_amdgcn_mbcnt_hi(~0u, __builtin_amdgcn_mbcnt_lo(~0u, 0u));
  PIN(ln2);
  const int hoff = ((((ln2 >> 4) & 1) * 144 + ((fg * 8 + w) >> 1)) * 64 + (2 * (w & 1) + (ln2 >> 5)) * 16 + (ln2 & 15)) * 16;
  auto h_store = [&](const float (&hv)[8], int tile) {
    unsigned o0 = pk_bf16(hv[0], hv[1]), o1 = pk_bf16(hv[2], hv[3]), p0 = pk_bf16(hv[4], hv[5]), p1 = pk_bf16(hv[6], hv[7]);
    auto s0 = __builtin_amdgcn_permlane16_swap(o0, p0, false, false);
    auto s1 = __builtin_amdgcn_permlane16_swap(o1, p1, false, false);
    u32x4 o = {s0[0], s1[0], s0[1], s1[1]};
    __builtin_amdgcn_raw_buffer_store_b128(o, hrs, hoff + tile * (2 * 144 * 1024), 0, 16);
  };

  constexpr int PF = K1PF;
  int sto = STB + (ln2 & 15) * 16;
  bf16x8 xh[PF];
  static_for<0, 2>([&](auto tc) {
    constexpr int tt = decltype(tc)::value;
    const Acc& ap = tt ? accA : accC;
    const f32x4 st0 = *(const f32x4*)(smem + sto), st1 = *(const f32x4*)(smem + sto + 256);
    sto += 512;
    float hv[8];
    static_for<0, 8>([&](auto kc) {
      constexpr int k = decltype(kc)::value, sb = 18 - LAG + tt * 4 + (k >> 1), hf = k & 1;
      if constexpr (sb == 0) MFMA_NEW(accB.v[hf][0], W[sb][0], xp_[4 + hf]); else MFMA_ACC(accB.v[hf][0], W[sb][0], xp_[4 + hf]); if constexpr (sb == 0) MFMA_NEW(accB.v[hf][1], W[sb][1], xp_[4 + hf]); else MFMA_ACC(accB.v[hf][1], W[sb][1], xp_[4 + hf]);
      if constexpr (sb + 1 < 18) xp_[4 + hf] = lds_b128(smem + XADDR(2 * (sb + 1) + hf));
      if constexpr (tt == 1 && k >= 8 - PF) xh[k - (8 - PF)] = lds_b128(smem + XT + XADDR(k - (8 - PF)));
      constexpr int h2 = k >> 2, r = k & 3;
      const f32x4& st = h2 ? st1 : st0;
      float g = __builtin_fmaf(st[0], cs[0][r], ap.v[h2][0][r]); PIN(g);
      float u = __builtin_fmaf(st[0], cs[1][r], ap.v[h2][1][r]); PIN(u);
      const float e = __builtin_amdgcn_exp2f(g * st[1]);
      hv[k] = (g * u) * __builtin_amdgcn_rcpf(__builtin_fmaf(e, st[2], st[2])); PIN(hv[k]);
      SB();
    });
    h_store(hv, t0 + tt);
    SB();
  });
  make_xsrc();
  int cur = XT;
#pragma unroll
  for (int v = 0; v < 4; ++v) xa[v] += XT;
  auto body = [&](int i, const Acc& ap, Acc& an) {
    bf16x8 xb[36 + PF];
#pragma unroll
    for (int n = 0; n < PF; ++n) xb[n] = xh[n];
    const f32x4 st0 = *(const f32x4*)(smem + sto), st1 = *(const f32x4*)(smem + sto + 256);
    sto += 512;
    float gg[8], ex[8], pp[8], hv[8];
    const f32x4 zero4 = {0.f, 0.f, 0.f, 0.f};
    const int dst = cur >= 2 * XT ? cur - 2 * XT : cur + 2 * XT;
    const int dl = cur == 3 * XT ? -3 * XT : XT;
    const char* xg = i + 2 < nt ? (const char*)x + (size_t)(t0 + i + 2) * XTB
                                : (const char*)w2 + (size_t)(36 * (xcd & 3) + (slot * 2 + i + 2 - nt) % 36) * XTB;
    const unsigned xl = lds0 + dst + w * 4608;
    SB();
    static_for<0, 36>([&](auto nc) {
      constexpr int n = decltype(nc)::value;
      constexpr int ks = n >> 1, hf = n & 1;
      if constexpr (ks == 0) { MFMA_NEW(an.v[hf][0], W[ks][0], xb[n]); MFMA_NEW(an.v[hf][1], W[ks][1], xb[n]); }
      else { MFMA_ACC(an.v[hf][0], W[ks][0], xb[n]); MFMA_ACC(an.v[hf][1], W[ks][1], xb[n]); }
      if constexpr (n + PF == 36) {
#pragma unroll
        for (int v = 0; v < 4; ++v) { xa[v] += dl; PIN(xa[v]); }
      }
      xb[n + PF] = lds_b128(smem + XADDR((n + PF) % 36));
      {
      if constexpr (n >= 2 && n < 26 && (n - 2) % 3 == 0) {
        constexpr int j = (n - 2) / 3, h2 = j >> 2, r = j & 3;
        const f32x4& st = h2 ? st1 : st0;
        gg[j] = __builtin_fmaf(st[0], cs[0][r], ap.v[h2][0][r]); PIN(gg[j]);
        pp[j] = __builtin_fmaf(st[0], cs[1][r], ap.v[h2][1][r]); PIN(pp[j]);
        ex[j] = gg[j] * st[1]; PIN(ex[j]);
      }
      if constexpr (n >= 3 && n < 27 && (n - 3) % 3 == 0) {
        constexpr int j = (n - 3) / 3;
        ex[j] = __builtin_amdgcn_exp2f(ex[j]); PIN(ex[j]);
        pp[j] = gg[j] * pp[j]; PIN(pp[j]);
      }
      if constexpr (n >= 4 && n < 28 && (n - 4) % 3 == 0) {
        constexpr int j = (n - 4) / 3;
        const float k2 = (j >> 2) ? st1[2] : st0[2];
        ex[j] = __builtin_fmaf(ex[j], k2, k2); PIN(ex[j]);
        ex[j] = __builtin_amdgcn_rcpf(ex[j]); PIN(ex[j]);
      }
      if constexpr (n >= 6 && n < 30 && (n - 6) % 3 == 0) { constexpr int j = (n - 6) / 3; hv[j] = pp[j] * ex[j]; PIN(hv[j]); }
      }
      if constexpr (n == 29) h_store(hv, t0 + i - 1);
      if constexpr (n % 6 == 1 && n / 6 < 5) glds16a<0>(xg, xsrc[n / 6], xl + (n / 6) * 1024);
      if constexpr (n == 30) {
        WAIT_V(7);
        BAR();
      }
      SB();
    });
#pragma unroll
    for (int n = 0; n < PF; ++n) xh[n] = xb[36 + n];
    cur += dl;
  };
  auto plain_epilogue = [&](const Acc& ap, int tile) {
    asm volatile("s_nop 15" ::: "memory");
    SB();
    const f32x4 st0 = *(const f32x4*)(smem + sto), st1 = *(const f32x4*)(smem + sto + 256);
    sto += 512;
    float hv[8];
#pragma unroll
    for (int j = 0; j < 8; ++j) {
      const int h2 = j >> 2, r = j & 3;
      const f32x4& st = h2 ? st1 : st0;
      const float g = __builtin_fmaf(st[0], cs[0][r], ap.v[h2][0][r]), u = __builtin_fmaf(st[0], cs[1][r], ap.v[h2][1][r]);
      const float e = __builtin_amdgcn_exp2f(g * st[1]);
      hv[j] = (g * u) * __builtin_amdgcn_rcpf(__builtin_fmaf(e, st[2], st[2]));
    }
    h_store(hv, tile);
  };
  for (int i = 3;; i += 2) {
    body(i, accB, accA);
    if (i + 1 >= nt) { plain_epilogue(accA, t0 + nt - 1); break; }
    body(i + 1, accA, accB);
    if (i + 2 >= nt) { plain_epilogue(accB, t0 + nt - 1); break; }
  }
  WAIT_V(0);
  WAIT_L0();
  BAR();
  if (tid == 0) __hip_atomic_store((GAS unsigned*)(ws + WS_DONE) + L, tag, RLX_AGENT);
  if (slot < 24) phase2(smem, x, w2, ws, out, tag, w);
}

extern "C" void kernel_launch(void* const* d_in, const int* in_sizes, int n_in, void* d_out, int out_size, void* d_ws, size_t ws_size,
                              hipStream_t stream) {
  const u16* x = (const u16*)d_in[0];
  const u16* kin = (const u16*)d_in[1];
  const u16* vin = (const u16*)d_in[2];
  const u16* sk = (const u16*)d_in[3];
  const u16* hk = (const u16*)d_in[4];
  const u16* dv = (const u16*)d_in[5];
  const u16* w1 = (const u16*)d_in[7];
  const u16* w3 = (const u16*)d_in[8];
  const u16* w2 = (const u16*)d_in[9];
  k1<<<256, 512, 0, stream>>>(x, w1, w3, kin, vin, sk, hk, dv, w2, (char*)d_ws, (u16*)d_out);
}
```
